```python
import torch

def _softmax(x, axis=-1):
    return torch.softmax(x, dim=axis)
def _gelu(x):
    return torch.nn.functional.gelu(x, approximate="tanh")
def _silu(x):
    return torch.nn.functional.silu(x)
def _sigmoid(x):
    return torch.sigmoid(x)
def _layernorm(x, axis=-1, eps=1e-5):
    m = x.mean(dim=axis, keepdim=True)
    v = x.var(dim=axis, keepdim=True, unbiased=False)
    return (x - m) / torch.sqrt(v + eps)
def _scatter_add(tgt, idx, src, axis=0):
    return tgt.clone().index_add_(axis, idx, src)
def _segment_sum(src, seg, n):
    out = torch.zeros((n,) + src.shape[1:], dtype=src.dtype)
    return out.index_add_(0, seg, src)
def _pad(x, pw):
    flat = tuple(v for pair in reversed(pw) for v in pair)
    return torch.nn.functional.pad(x, flat)

kernel_name = 'gated_mix__ssm_conv_h3__geglu_ffn__prompt'

def setup_inputs():

    _w = (lambda a: (lambda t: t.to(torch.bfloat16) if t.is_floating_point() else t)(torch.as_tensor(a)))
    def _A_setup():
        B, S, D = 1, 2048, 1024
        ks = 4
        return {
            "q": _w((lambda *s: torch.randn(*s, dtype=torch.bfloat16) * 0.05)(B, S, D)),
            "k": _w((lambda *s: torch.randn(*s, dtype=torch.bfloat16) * 0.4)(B, S, D)),
            "v": _w((lambda *s: torch.randn(*s, dtype=torch.bfloat16) * 0.4)(B, S, D)),
            "shift_k": _w((lambda *s: torch.randn(*s, dtype=torch.bfloat16) * 0.4)(D, ks) * 0.5),
            "ssm_k": _w((lambda *s: torch.randn(*s, dtype=torch.bfloat16) * 0.4)(D, S) * (1.0 / S ** 0.5)),
            "ssm_D": _w((lambda *s: torch.randn(*s, dtype=torch.bfloat16) * 0.4)(D)),
        }
    _A_in = _A_setup()

    def _B_setup():
        B, S, D, F = 1, 2048, 1024, 3584
        return {
            "x": _w((lambda *s: torch.randn(*s, dtype=torch.bfloat16) * 0.05)(B, S, D)),
            "w_gate": _w((lambda *s: torch.randn(*s, dtype=torch.bfloat16) * 0.05)(D, F)),
            "w_up": _w((lambda *s: torch.randn(*s, dtype=torch.bfloat16) * 0.05)(D, F)),
            "w_down": _w((lambda *s: torch.randn(*s, dtype=torch.bfloat16) * 0.05)(F, D)),
        }
    _B_in = _B_setup()

    return {
        "A_q": _A_in["q"],
        "A_k": _A_in["k"],
        "A_v": _A_in["v"],
        "A_shift_k": _A_in["shift_k"],
        "A_ssm_k": _A_in["ssm_k"],
        "A_ssm_D": _A_in["ssm_D"],
        "B_x": _B_in["x"],
        "B_w_gate": _B_in["w_gate"],
        "B_w_up": _B_in["w_up"],
        "B_w_down": _B_in["w_down"],
        "gate_w": _w((lambda *s: torch.randn(*s, dtype=torch.bfloat16) * 0.05)(1024) * 16.0),
    }

def reference(A_q, A_k, A_v, A_shift_k, A_ssm_k, A_ssm_D, B_x, B_w_gate, B_w_up, B_w_down, gate_w):
    def _A_ref(q, k, v, shift_k, ssm_k, ssm_D):
        B, S, D = q.shape
        ks = shift_k.shape[-1]
        k_pad = _pad(k, ((0, 0), (ks - 1, 0), (0, 0)))
        k_shift = torch.zeros_like(k)
        for t in range(ks):
            k_shift = k_shift + k_pad[:, t:t + S, :] * shift_k[:, ks - 1 - t][None, None, :]
        kv = k_shift * v
        L = 2 * S
        kv_pad = _pad(kv, ((0, 0), (0, S), (0, 0)))
        kf_pad = _pad(ssm_k, ((0, 0), (0, S)))
        kv_f = (lambda x, axes=(-2,-1): torch.fft.rfft2(x.float(), dim=axes))(kv_pad[:, None, :, :], axes=(1, 2))
        kf_f = (lambda x, axes=(-2,-1): torch.fft.rfft2(x.float(), dim=axes))(kf_pad[:, None, :], axes=(1, 2))
        kf_f = (lambda x, *d: x.permute(*d))(kf_f, 1, 2, 0)
        y_f = kv_f * kf_f[None, :, :, :]
        y = (lambda x, s, axes=(-2,-1): torch.fft.irfft2(x, s=s, dim=axes).to(torch.bfloat16))(y_f, (1, L), axes=(1, 2))[:, 0, :S, :]
        y = y + kv * ssm_D[None, None, :]
        return q * y

    def _B_ref(x, w_gate, w_up, w_down):
        return (_gelu(x @ w_gate) * (x @ w_up)) @ w_down

    x = A_q
    g = _sigmoid((x * gate_w).sum(-1, keepdims=True))
    a = _A_ref(x, A_k, A_v, A_shift_k, A_ssm_k, A_ssm_D)
    b = _B_ref(x, B_w_gate, B_w_up, B_w_down)
    return g * a + (1 - g) * b
```

```cpp
#include <hip/hip_runtime.h>
#include <hip/hip_bf16.h>

#define LAS __attribute__((address_space(3)))
#define GAS __attribute__((address_space(1)))
typedef unsigned short u16;
typedef short bf16x8 __attribute__((ext_vector_type(8)));
typedef short bf16x4 __attribute__((ext_vector_type(4)));
typedef float f32x4 __attribute__((ext_vector_type(4)));
typedef float f32x2 __attribute__((ext_vector_type(2)));
typedef unsigned u32x4 __attribute__((ext_vector_type(4)));
typedef unsigned u32x2 __attribute__((ext_vector_type(2)));
typedef GAS unsigned gu32;
typedef GAS unsigned long long gu64;
#define RLX_AGENT __ATOMIC_RELAXED, __HIP_MEMORY_SCOPE_AGENT

constexpr int S_ = 2048, D_ = 1024, F_ = 3584;
constexpr int URP = 2112;
constexpr size_t WS_H = 0;
constexpr size_t WS_UR = WS_H + (size_t)S_ * F_ * 2;
constexpr size_t WS_G = WS_UR + (size_t)D_ * URP * 2;
constexpr size_t WS_Y2 = WS_G + (size_t)S_ * 4;
constexpr size_t WS_X = WS_Y2 + (size_t)256 * S_ * 8;
constexpr size_t WS_FL = WS_X + (size_t)256 * 32768;
constexpr int K1_LDS = 131072, K2_LDS = 163840;

__device__ __forceinline__ unsigned cvt_pk_bf16(float lo, float hi) {
    unsigned r; asm("v_cvt_pk_bf16_f32 %0, %1, %2" : "=v"(r) : "v"(lo), "v"(hi)); return r;
}
__device__ __forceinline__ float bf_lo(unsigned w) { return __uint_as_float(w << 16); }
__device__ __forceinline__ float bf_hi(unsigned w) { return __uint_as_float(w & 0xffff0000u); }
__device__ __forceinline__ float rbf(float x) { return bf_lo(cvt_pk_bf16(x, x)); }

#define GLDS16(gptr, ldsoff) __builtin_amdgcn_global_load_lds((const unsigned*)(gptr), (LAS unsigned*)(lds + (ldsoff)), 16, 0, 0)
#define LDS_B128(off) (*(const LAS bf16x8*)(lds + (off)))
#define LDS_TR(dst, vaddr, imm) asm volatile("ds_read_b64_tr_b16 %0, %1 offset:%2" : "=v"(dst) : "v"(vaddr), "i"(imm) : "memory")
#define CAT8(a, b) __builtin_shufflevector(a, b, 0, 1, 2, 3, 4, 5, 6, 7)
#define WAIT_V(n) asm volatile("s_waitcnt vmcnt(" #n ")" ::: "memory")
#define WAIT_L(n) asm volatile("s_waitcnt lgkmcnt(" #n ")" ::: "memory")
#define BAR() do { asm volatile("" ::: "memory"); __builtin_amdgcn_s_barrier(); asm volatile("" ::: "memory"); } while (0)
#define SCHED() __builtin_amdgcn_sched_barrier(0)

__device__ __forceinline__ float gelu_mul(float g, float u) {
    g = rbf(g); u = rbf(u);
    const float p = g * g * (-0.044715f * 2.3022082f) + (-2.3022082f);
    const float e = __builtin_amdgcn_exp2f(g * p);
    const float ge = rbf(g * __builtin_amdgcn_rcpf(1.0f + e));
    return ge * u;
}

__device__ __forceinline__ void prep_block(LAS unsigned char* lds, int j, const u16* x, const u16* kk, const u16* vv,
                                           const u16* shk, const u16* gw, unsigned char* ws) {
    const int tid = threadIdx.x, wid = tid >> 6, lane = tid & 63;
    if (tid < 16) *(u32x4*)(ws + WS_FL + (size_t)(j * 16 + tid) * 64) = (u32x4){0u, 0u, 0u, 0u};
    if (tid >= 256) { const int t = tid - 256; *(u32x4*)(ws + WS_UR + ((size_t)(j * 32 + (t >> 3)) * URP + 2048) * 2 + (t & 7) * 16) = (u32x4){0u, 0u, 0u, 0u}; }
    const int cg = tid & 127, rg = tid >> 7;
    float w[8][4];
    {
        const u32x4* wp = (const u32x4*)(shk + cg * 32);
#pragma unroll
        for (int i = 0; i < 4; ++i) { const u32x4 t = wp[i];
            w[2 * i][0] = bf_lo(t.x); w[2 * i][1] = bf_hi(t.x); w[2 * i][2] = bf_lo(t.y); w[2 * i][3] = bf_hi(t.y);
            w[2 * i + 1][0] = bf_lo(t.z); w[2 * i + 1][1] = bf_hi(t.z); w[2 * i + 1][2] = bf_lo(t.w); w[2 * i + 1][3] = bf_hi(t.w); }
    }
#pragma unroll
    for (int hh = 0; hh < 2; ++hh) {
        const int s0 = 64 * j + 16 * rg + 8 * hh;
        u32x4 kr[11], vr[8];
#pragma unroll
        for (int i = 0; i < 11; ++i) { const int s = s0 - 3 + i; kr[i] = (s >= 0) ? *(const u32x4*)(kk + (size_t)s * D_ + cg * 8) : (u32x4){0u, 0u, 0u, 0u}; }
#pragma unroll
        for (int i = 0; i < 8; ++i) vr[i] = *(const u32x4*)(vv + (size_t)(s0 + i) * D_ + cg * 8);
#pragma unroll
        for (int c = 0; c < 8; ++c) {
            float kf[11], o[8];
#pragma unroll
            for (int i = 0; i < 11; ++i) { const unsigned q = kr[i][c >> 1]; kf[i] = (c & 1) ? bf_hi(q) : bf_lo(q); }
#pragma unroll
            for (int i = 0; i < 8; ++i) { const unsigned q = vr[i][c >> 1]; const float vf = (c & 1) ? bf_hi(q) : bf_lo(q);
                float a = kf[i] * w[c][3]; a = fmaf(kf[i + 1], w[c][2], a); a = fmaf(kf[i + 2], w[c][1], a); a = fmaf(kf[i + 3], w[c][0], a);
                o[i] = rbf(a) * vf; }
            u32x4 pk; pk.x = cvt_pk_bf16(o[7], o[6]); pk.y = cvt_pk_bf16(o[5], o[4]); pk.z = cvt_pk_bf16(o[3], o[2]); pk.w = cvt_pk_bf16(o[1], o[0]);
            const int chunk = 7 - (2 * rg + hh);
            *(LAS u32x4*)(lds + (cg * 8 + c) * 128 + ((chunk ^ (cg & 7)) * 16)) = pk;
        }
    }
    {
        const u32x4 g0 = *(const u32x4*)(gw + lane * 8), g1 = *(const u32x4*)(gw + 512 + lane * 8);
#pragma unroll
        for (int r = 0; r < 8; ++r) {
            const int s = 64 * j + wid * 8 + r;
            const u32x4 a0 = *(const u32x4*)(x + (size_t)s * D_ + lane * 8), a1 = *(const u32x4*)(x + (size_t)s * D_ + 512 + lane * 8);
            float acc = 0.f;
#pragma unroll
            for (int i = 0; i < 4; ++i) {
                acc += rbf(bf_lo(a0[i]) * bf_lo(g0[i])); acc += rbf(bf_hi(a0[i]) * bf_hi(g0[i]));
                acc += rbf(bf_lo(a1[i]) * bf_lo(g1[i])); acc += rbf(bf_hi(a1[i]) * bf_hi(g1[i]));
            }
#pragma unroll
            for (int o = 32; o >= 1; o >>= 1) acc += __shfl_xor(acc, o);
            if (lane == 0) { const float z = rbf(acc); ((float*)(ws + WS_G))[s] = rbf(1.0f / (1.0f + __expf(-z))); }
        }
    }
    __syncthreads();
    const size_t xb = (size_t)(1984 - 64 * j) * 2;
#pragma unroll 4
    for (int it = 0; it < 16; ++it) {
        const int ch = it * 64 + (tid >> 3), c = tid & 7;
        const u32x4 t = *(const LAS u32x4*)(lds + ch * 128 + ((c ^ ((ch >> 3) & 7)) * 16));
        *(u32x4*)(ws + WS_UR + (size_t)ch * URP * 2 + xb + c * 16) = t;
    }
}

__global__ __launch_bounds__(512, 2) void k1_gemm1_prep(const u16* __restrict__ x, const u16* __restrict__ kk, const u16* __restrict__ vv,
                                                        const u16* __restrict__ shk, const u16* __restrict__ wg, const u16* __restrict__ wu,
                                                        const u16* __restrict__ gw, unsigned char* __restrict__ ws) {
    extern __shared__ __attribute__((aligned(16))) unsigned char smem[];
    LAS unsigned char* lds = (LAS unsigned char*)smem;
    const int xg = blockIdx.x & 7, l = blockIdx.x >> 3;
    if (l >= 28) { prep_block(lds, xg * 4 + (l - 28), x, kk, vv, shk, gw, ws); return; }
    const int pm = 4 * (xg & 1) + (l & 3), pn = 7 * (xg >> 1) + (l >> 2);
    const int tid = threadIdx.x, wid = __builtin_amdgcn_readfirstlane(tid >> 6), lane = tid & 63, wr = wid >> 2, wc = wid & 3, fr = lane & 15, fq = lane >> 4;
    constexpr int HTB = 16384;
    unsigned voffA[2], voffB[2];
#pragma unroll
    for (int i = 0; i < 2; ++i) {
        const int piece = wid + 8 * i;
        { const int row = piece * 8 + (lane >> 3), ch = (lane & 7) ^ (lane >> 3); voffA[i] = (unsigned)(row * 2048 + ch * 16); }
        { const int kr = piece * 4 + (lane >> 4), fs = (kr & 3) | (((kr >> 3) & 1) << 2), ch = (lane & 15) ^ (2 * fs); voffB[i] = (unsigned)(kr * (F_ * 2) + ch * 16); }
    }
    const unsigned ldsw = (unsigned)wid * 1024u;
    const int aoff0 = ((wr * 64 + fr) >> 3) * 1024 + (fr & 7) * 128 + ((fq ^ (fr & 7)) * 16), aoff1 = aoff0 ^ 64;
    const int tq = (lane & 15) >> 2, tp = lane & 3;
    const unsigned boff0 = (unsigned)(size_t)lds + 4 * HTB + (8 * fq + tq) * 256 + (((4 * wc + (tp >> 1)) ^ (2 * (tq | ((fq & 1) << 2)))) * 16) + 8 * (tp & 1), boff1 = boff0 ^ 32;
    const char* cA = (const char*)x + (size_t)pm * 256 * 2048;
    const char* cG = (const char*)wg + (size_t)pn * 256;
    const char* cU = (const char*)wu + (size_t)pn * 256;
    constexpr size_t kA = 128, kB = (size_t)64 * F_ * 2, hA = (size_t)128 * 2048;
#define SA(b, h) (((b) * 2 + (h)) * HTB)
#define SB(b, h) ((4 + (b) * 2 + (h)) * HTB)
#define STAGE(bufoff, gbase, voff) do { _Pragma("unroll") for (int _i = 0; _i < 2; ++_i) GLDS16((gbase) + (voff)[_i], (bufoff) + ldsw + _i * 8192); } while (0)
#define LDA(dst, b, h) do { _Pragma("unroll") for (int m = 0; m < 4; ++m) { dst[m][0] = LDS_B128(SA(b, h) + aoff0 + m * 2048); dst[m][1] = LDS_B128(SA(b, h) + aoff1 + m * 2048); } } while (0)
#define LDB(dst, b, h) do { \
        LDS_TR(dst[0], boff0, ((b) * 2 + (h)) * HTB); LDS_TR(dst[1], boff0, ((b) * 2 + (h)) * HTB + 1024); \
        LDS_TR(dst[2], boff0, ((b) * 2 + (h)) * HTB + 8192); LDS_TR(dst[3], boff0, ((b) * 2 + (h)) * HTB + 9216); \
        LDS_TR(dst[4], boff1, ((b) * 2 + (h)) * HTB); LDS_TR(dst[5], boff1, ((b) * 2 + (h)) * HTB + 1024); \
        LDS_TR(dst[6], boff1, ((b) * 2 + (h)) * HTB + 8192); LDS_TR(dst[7], boff1, ((b) * 2 + (h)) * HTB + 9216); } while (0)
#define WAIT_B(B) asm volatile("s_waitcnt lgkmcnt(0)" : "+v"(B[0]), "+v"(B[1]), "+v"(B[2]), "+v"(B[3]), "+v"(B[4]), "+v"(B[5]), "+v"(B[6]), "+v"(B[7]) :: "memory")
#define MMA(ai, bj, At, Bt) do { __builtin_amdgcn_s_setprio(1); _Pragma("unroll") for (int m = 0; m < 4; ++m) _Pragma("unroll") for (int n = 0; n < 2; ++n) _Pragma("unroll") for (int k = 0; k < 2; ++k) \
        acc[ai][bj][m][n] = __builtin_amdgcn_mfma_f32_16x16x32_bf16(CAT8(Bt[4 * n + 2 * k], Bt[4 * n + 2 * k + 1]), At[m][k], acc[ai][bj][m][n], 0, 0, 0); __builtin_amdgcn_s_setprio(0); } while (0)
    f32x4 acc[2][2][4][2];
#pragma unroll
    for (int a = 0; a < 2; ++a)
#pragma unroll
        for (int b = 0; b < 2; ++b)
#pragma unroll
            for (int m = 0; m < 4; ++m)
#pragma unroll
                for (int n = 0; n < 2; ++n) acc[a][b][m][n] = (f32x4){0.f, 0.f, 0.f, 0.f};
    bf16x8 At[4][2]; bf16x4 B0[8], B1[8];
    constexpr int nt = D_ / 64;
    STAGE(SB(0, 0), cG, voffB); STAGE(SA(0, 0), cA, voffA); STAGE(SB(0, 1), cU, voffB); STAGE(SA(0, 1), cA + hA, voffA);
    if (wr == 1) BAR();
    WAIT_V(4); BAR();
    STAGE(SB(1, 0), cG + kB, voffB); STAGE(SA(1, 0), cA + kA, voffA); STAGE(SB(1, 1), cU + kB, voffB);
    WAIT_V(6); BAR();
    for (int t = 0; t < nt; t += 2) {
        const int t2 = (t + 2 < nt) ? t + 2 : 0;
        const char* a1 = cA + (size_t)(t + 1) * kA;
        const char* a2 = cA + (size_t)t2 * kA; const char* g2 = cG + (size_t)t2 * kB; const char* u2 = cU + (size_t)t2 * kB;
        const char* a3 = a2 + kA; const char* g3 = g2 + kB; const char* u3 = u2 + kB;
        LDB(B0, 0, 0); SCHED(); LDA(At, 0, 0); STAGE(SA(1, 1), a1 + hA, voffA);
        WAIT_L(8); BAR(); WAIT_B(B0); MMA(0, 0, At, B0); BAR(); SCHED();
        LDB(B1, 0, 1); STAGE(SB(0, 0), g2, voffB);
        BAR(); WAIT_B(B1); MMA(0, 1, At, B1); BAR();
        LDA(At, 0, 1); STAGE(SA(0, 0), a2, voffA);
        BAR(); WAIT_L(0); MMA(1, 0, At, B0); BAR(); SCHED();
        STAGE(SB(0, 1), u2, voffB);
        WAIT_V(6); BAR(); MMA(1, 1, At, B1); BAR();
        LDB(B0, 1, 0); SCHED(); LDA(At, 1, 0); STAGE(SA(0, 1), a2 + hA, voffA);
        WAIT_L(8); BAR(); WAIT_B(B0); MMA(0, 0, At, B0); BAR(); SCHED();
        LDB(B1, 1, 1); STAGE(SB(1, 0), g3, voffB);
        BAR(); WAIT_B(B1); MMA(0, 1, At, B1); BAR();
        LDA(At, 1, 1); STAGE(SA(1, 0), a3, voffA);
        BAR(); WAIT_L(0); MMA(1, 0, At, B0); BAR(); SCHED();
        STAGE(SB(1, 1), u3, voffB);
        WAIT_V(6); BAR(); MMA(1, 1, At, B1); BAR();
    }
    {
        u16* hp = (u16*)(ws + WS_H) + (size_t)(pm * 256 + wr * 64 + fr) * F_ + pn * 128 + wc * 32 + fq * 8;
#pragma unroll
        for (int ai = 0; ai < 2; ++ai)
#pragma unroll
            for (int m = 0; m < 4; ++m) {
                const f32x4 g0 = acc[ai][0][m][0], g1 = acc[ai][0][m][1], u0 = acc[ai][1][m][0], u1 = acc[ai][1][m][1];
                u32x4 o;
                o.x = cvt_pk_bf16(gelu_mul(g0[0], u0[0]), gelu_mul(g0[1], u0[1])); o.y = cvt_pk_bf16(gelu_mul(g0[2], u0[2]), gelu_mul(g0[3], u0[3]));
                o.z = cvt_pk_bf16(gelu_mul(g1[0], u1[0]), gelu_mul(g1[1], u1[1])); o.w = cvt_pk_bf16(gelu_mul(g1[2], u1[2]), gelu_mul(g1[3], u1[3]));
                *(u32x4*)(hp + (size_t)(ai * 128 + m * 16) * F_) = o;
            }
    }
    WAIT_V(0);
    if (wr == 0) BAR();
#undef SA
#undef SB
#undef STAGE
#undef LDA
#undef LDB
#undef MMA
#undef WAIT_B
}

constexpr int CV_UP = 4160, CV_HP = 4640, CV_HB = 4 * CV_UP, CV_CH = CV_HB + 2 * CV_HP;
constexpr int CV_T = 106496;
constexpr unsigned kSpinCap = 1u << 22;

__global__ __launch_bounds__(512, 2) void k2_conv_gemm2(const u16* __restrict__ q, const u16* __restrict__ ssmk, const u16* __restrict__ ssmD,
                                                        const u16* __restrict__ wd, unsigned char* __restrict__ ws, u16* __restrict__ out) {
    extern __shared__ __attribute__((aligned(16))) unsigned char smem[];
    LAS unsigned char* lds = (LAS unsigned char*)smem;
    const int tid = threadIdx.x, wid = __builtin_amdgcn_readfirstlane(tid >> 6), lane = tid & 63, fr = lane & 15, fq = lane >> 4;
    const int bid = blockIdx.x, xg = bid & 7, l = bid >> 3;
    const int z = xg & 1, tm = 8 * ((xg >> 1) & 1) + (l & 7), tn = 4 * (xg >> 2) + (l >> 3);
    const int kh = wid >> 2, wm = (wid >> 1) & 1, wn = wid & 1;
    unsigned voffA[2], voffB[2];
#pragma unroll
    for (int i = 0; i < 2; ++i) {
        const int piece = wid + 8 * i;
        { const int row = piece * 8 + (lane >> 3), ch = (lane & 7) ^ (lane >> 3); voffA[i] = (unsigned)(row * (F_ * 2) + ch * 16); }
        { const int kr = piece * 4 + (lane >> 4), ch = (lane & 15) ^ (2 * (kr & 7)); voffB[i] = (unsigned)(kr * 2048 + ch * 16); }
    }
    const unsigned ldsw = (unsigned)wid * 1024u;
    const char* cA = (const char*)(ws + WS_H) + (size_t)tm * 128 * F_ * 2 + (size_t)z * 1792 * 2;
    const char* cB = (const char*)wd + (size_t)z * 1792 * 2048 + (size_t)tn * 256;
    constexpr size_t kA = 128, kB = (size_t)64 * 2048;
#define SLOT(i) (32768 + (i) * 32768)
#define STAGE2(slot, t) do { const char* _a = cA + (size_t)(t) * kA; const char* _b = cB + (size_t)(t) * kB; \
        GLDS16(_a + voffA[0], SLOT(slot) + ldsw); GLDS16(_a + voffA[1], SLOT(slot) + ldsw + 8192); \
        GLDS16(_b + voffB[0], SLOT(slot) + 16384 + ldsw); GLDS16(_b + voffB[1], SLOT(slot) + 16384 + ldsw + 8192); } while (0)

    {
        const int ch = wid & 3, hf = wid >> 2, d = bid * 4 + ch, CB = ch * CV_CH;
        const char* ur = (const char*)(ws + WS_UR) + (size_t)d * URP * 2;
        const auto hrs = __builtin_amdgcn_make_buffer_rsrc((void*)ssmk, 0, D_ * S_ * 2, 0x00020000);
        const float dval = bf_lo(ssmD[d]);
        u32x2 h03 = *(const u32x2*)(ssmk + (size_t)d * S_);
#pragma unroll
        for (int c2 = 0; c2 < 2; ++c2) { const int c = 2 * hf + c2;
#pragma unroll
            for (int i = 0; i < 4; ++i) GLDS16(ur + (3 - c) * 2 + i * 1024 + lane * 16, CB + c * CV_UP + i * 1024); }
#pragma unroll
        for (int i = 0; i < 4; ++i)
            __builtin_amdgcn_raw_ptr_buffer_load_lds(hrs, (LAS void*)(lds + CB + CV_HB + hf * CV_HP + (29 - hf) * 16 + i * 1024), 16,
                                                     d * (S_ * 2) + (1 - hf) * 8 + i * 1024 + lane * 16, 0, 0, 0);
        STAGE2(3, 0);
        if (lane < 28) *(LAS u32x4*)(lds + CB + CV_HB + hf * CV_HP + lane * 16) = (u32x4){0u, 0u, 0u, 0u};
        else if (lane < 34) { const int e = lane - 28; *(LAS u32x4*)(lds + CB + (2 * hf + e / 3) * CV_UP + 4096 + (e % 3) * 16) = (u32x4){0u, 0u, 0u, 0u}; }
        else if (lane == 34 && hf == 0) *(LAS u32x4*)(lds + CB + CV_HB + 28 * 16) = (u32x4){0u, 0u, h03.x, h03.y};
        WAIT_V(4); WAIT_L(0); BAR();
        const int ua = CB + (fr & 3) * CV_UP + 2 * (2040 - 8 * (fr >> 2) + 8 * fq) - 64 * 59 - 256 * hf;
        const int ha = CB + CV_HB + (fr & 1) * CV_HP + 2 * (224 + 32 * (fr >> 1) + 8 * fq) - 64 * 7 - 256 * hf + 256;
        f32x4 acc[8];
#pragma unroll
        for (int t = 0; t < 8; ++t) acc[t] = (f32x4){0.f, 0.f, 0.f, 0.f};
#pragma unroll
        for (int cc = 0; cc < 4; ++cc) {
            bf16x8 U[8], Hf[8];
#pragma unroll
            for (int a = 0; a < 8; ++a) { U[a] = LDS_B128(ua + 64 * (59 - 8 * a - cc)); Hf[a] = LDS_B128(ha + 64 * (8 * a - cc + 3)); }
#pragma unroll
            for (int mx = 0; mx < 8; ++mx) {
#pragma unroll
                for (int o = 0; o <= mx; ++o) {
                    if (mx + o <= 7) acc[mx + o] = __builtin_amdgcn_mfma_f32_16x16x32_bf16(U[mx], Hf[o], acc[mx + o], 0, 0, 0);
                    if (o < mx && mx + o <= 7) acc[mx + o] = __builtin_amdgcn_mfma_f32_16x16x32_bf16(U[o], Hf[mx], acc[mx + o], 0, 0, 0);
                }
            }
        }
        WAIT_L(0); BAR();
        const int xw = CB + CV_HB + hf * CV_HP + lane * 16, xr = CB + CV_HB + (1 - hf) * CV_HP + lane * 16;
#pragma unroll
        for (int t = 0; t < 4; ++t) *(LAS f32x4*)(lds + xw + t * 1024) = hf ? acc[t] : acc[4 + t];
        WAIT_L(0); BAR();
#pragma unroll
        for (int t = 0; t < 4; ++t) {
            const f32x4 y = (hf ? acc[4 + t] : acc[t]) + *(const LAS f32x4*)(lds + xr + t * 1024);
            const int s0 = 256 * (4 * hf + t) + 32 * (fr >> 1) + 4 * (fr & 1) + 8 * fq;
            const u32x2 kv = *(const LAS u32x2*)(lds + CB + 3 * CV_UP + 2 * (2044 - s0));
            const float k3 = bf_lo(kv.x), k2 = bf_hi(kv.x), k1 = bf_lo(kv.y), k0 = bf_hi(kv.y);
            const unsigned p01 = cvt_pk_bf16(rbf(y[0]) + rbf(k0 * dval), rbf(y[1]) + rbf(k1 * dval));
            const unsigned p23 = cvt_pk_bf16(rbf(y[2]) + rbf(k2 * dval), rbf(y[3]) + rbf(k3 * dval));
            LAS u16* tp = (LAS u16*)(lds + CV_T + s0 * 8 + ch * 2);
            tp[0] = (u16)p01; tp[4] = (u16)(p01 >> 16); tp[8] = (u16)p23; tp[12] = (u16)(p23 >> 16);
        }
        WAIT_L(0); BAR();
        const auto yrs = __builtin_amdgcn_make_buffer_rsrc((void*)(ws + WS_Y2 + (size_t)bid * 16384), 0, 16384, 0x00020000);
#pragma unroll
        for (int i = 0; i < 2; ++i) {
            const u32x4 t = *(const LAS u32x4*)(lds + CV_T + i * 8192 + tid * 16);
            __builtin_amdgcn_raw_buffer_store_b128(t, yrs, i * 8192 + tid * 16, 0, 16);
        }
    }
    STAGE2(0, 1); STAGE2(1, 2);
    const int aoff = ((wm * 64 + fr) >> 3) * 1024 + (fr & 7) * 128 + (((4 * kh + fq) ^ (fr & 7)) * 16);
    const int tq = fr >> 2, tp4 = lane & 3;
    const unsigned boffb = (unsigned)(size_t)lds + SLOT(0) + 16384 + (32 * kh + 4 * fq + tq) * 256 + 8 * (tp4 & 1);
    unsigned boff[2][4];
#pragma unroll
    for (int n = 0; n < 4; ++n) { boff[0][n] = boffb + (((8 * wn + 2 * n + (tp4 >> 1)) ^ (2 * ((4 * fq + tq) & 7))) * 16); boff[1][n] = boff[0][n] + 65536; }
    f32x4 acc[4][4];
#pragma unroll
    for (int m = 0; m < 4; ++m)
#pragma unroll
        for (int n = 0; n < 4; ++n) acc[m][n] = (f32x4){0.f, 0.f, 0.f, 0.f};
    bf16x8 Aa[4], Ab[4]; bf16x4 Ba[8], Bb[8];
#define LDF(A_, B_, slot) do { _Pragma("unroll") for (int i = 0; i < 4; ++i) { \
        LDS_TR(B_[2 * i], boff[(slot) >> 1][i], ((slot) & 1) * 32768); LDS_TR(B_[2 * i + 1], boff[(slot) >> 1][i], ((slot) & 1) * 32768 + 4096); \
        A_[i] = LDS_B128(SLOT(slot) + aoff + i * 2048); } } while (0)
#define MM2(A_, B_) do { _Pragma("unroll") for (int m = 0; m < 4; ++m) _Pragma("unroll") for (int n = 0; n < 4; ++n) \
        acc[m][n] = __builtin_amdgcn_mfma_f32_16x16x32_bf16(CAT8(B_[2 * n], B_[2 * n + 1]), A_[m], acc[m][n], 0, 0, 0); SCHED(); } while (0)
#define WAIT_F(A, B) asm volatile("s_waitcnt lgkmcnt(0)" : "+v"(B[0]), "+v"(B[1]), "+v"(B[2]), "+v"(B[3]), "+v"(B[4]), "+v"(B[5]), "+v"(B[6]), "+v"(B[7]), \
        "+v"(A[0]), "+v"(A[1]), "+v"(A[2]), "+v"(A[3]) :: "memory")
    WAIT_V(10); WAIT_L(0); BAR();
    STAGE2(2, 3);
    LDF(Aa, Ba, 3);
    gu32* flags = (gu32*)(ws + WS_FL);
    for (int t = 0; t < 24; t += 4) {
        WAIT_V(8); WAIT_F(Aa, Ba); BAR(); STAGE2(3, t + 4); LDF(Ab, Bb, 0); MM2(Aa, Ba);
        WAIT_V(8); WAIT_F(Ab, Bb); BAR(); STAGE2(0, t + 5); LDF(Aa, Ba, 1); MM2(Ab, Bb);
        WAIT_V(8); WAIT_F(Aa, Ba); BAR();
        if (t == 4 && tid == 0) __hip_atomic_store(flags + bid * 16, 1u, RLX_AGENT);
        STAGE2(1, t + 6); LDF(Ab, Bb, 2); MM2(Aa, Ba);
        WAIT_V(8); WAIT_F(Ab, Bb); BAR(); STAGE2(2, t + 7); LDF(Aa, Ba, 3); MM2(Ab, Bb);
    }
    WAIT_V(8); WAIT_F(Aa, Ba); BAR(); LDF(Ab, Bb, 0); MM2(Aa, Ba);
    WAIT_V(4); WAIT_F(Ab, Bb); BAR(); LDF(Aa, Ba, 1); MM2(Ab, Bb);
    WAIT_V(0); WAIT_F(Aa, Ba); BAR(); LDF(Ab, Bb, 2); MM2(Aa, Ba);
    WAIT_F(Ab, Bb); MM2(Ab, Bb);
    WAIT_L(0); BAR();
    {
        const int xw = wid * 8192 + lane * 16, xr = (wid ^ 4) * 8192 + lane * 16;
#pragma unroll
        for (int i = 0; i < 2; ++i)
#pragma unroll
            for (int n = 0; n < 4; ++n) *(LAS f32x4*)(lds + xw + (i * 4 + n) * 1024) = kh ? acc[2 * i][n] : acc[2 * i + 1][n];
        WAIT_L(0); BAR();
        f32x4 keep[4], send[4];
#pragma unroll
        for (int n = 0; n < 4; ++n) {
            const f32x4 lo = (kh ? acc[1][n] : acc[0][n]) + *(const LAS f32x4*)(lds + xr + n * 1024);
            const f32x4 hi = (kh ? acc[3][n] : acc[2][n]) + *(const LAS f32x4*)(lds + xr + (4 + n) * 1024);
            keep[n] = z ? hi : lo; send[n] = z ? lo : hi;
        }
        const auto xs = __builtin_amdgcn_make_buffer_rsrc((void*)(ws + WS_X + (size_t)bid * 32768), 0, 32768, 0x00020000);
#pragma unroll
        for (int n = 0; n < 4; ++n) __builtin_amdgcn_raw_buffer_store_b128(__builtin_bit_cast(u32x4, send[n]), xs, wid * 4096 + n * 1024 + lane * 16, 0, 16);
        const int s = tm * 128 + wm * 64 + (2 * z + kh) * 16 + fr, dcol = tn * 128 + wn * 64 + 4 * fq;
        u32x2 qv[4];
#pragma unroll
        for (int n = 0; n < 4; ++n) qv[n] = *(const u32x2*)(q + (size_t)s * D_ + dcol + 16 * n);
        const float g = ((const float*)(ws + WS_G))[s];
        WAIT_V(0); BAR();
        if (tid == 0) __hip_atomic_store(flags + (256 + bid) * 16, 1u, RLX_AGENT);
        if (wid == 0) {
            gu32* fp = flags + ((lane < 32) ? (tn * 32 + lane) : (256 + (bid ^ 1))) * 16;
            for (unsigned spins = 0; spins < kSpinCap; ++spins) {
                const unsigned v = (lane <= 32) ? __hip_atomic_load(fp, RLX_AGENT) : 1u;
                if (__all(v == 1u)) break;
                __builtin_amdgcn_s_sleep(1);
            }
        }
        BAR();
        const auto xp = __builtin_amdgcn_make_buffer_rsrc((void*)(ws + WS_X + (size_t)(bid ^ 1) * 32768), 0, 32768, 0x00020000);
        const auto y2 = __builtin_amdgcn_make_buffer_rsrc((void*)(ws + WS_Y2), 0, 256 * S_ * 8, 0x00020000);
        f32x4 pr[4]; u32x2 yv[4];
#pragma unroll
        for (int n = 0; n < 4; ++n) {
            pr[n] = __builtin_bit_cast(f32x4, __builtin_amdgcn_raw_buffer_load_b128(xp, wid * 4096 + n * 1024 + lane * 16, 0, 16));
            yv[n] = __builtin_amdgcn_raw_buffer_load_b64(y2, (((dcol >> 2) + 4 * n) * S_ + s) * 8, 0, 16);
        }
        const float omg = rbf(1.0f - g);
#pragma unroll
        for (int n = 0; n < 4; ++n) {
            const f32x4 b = keep[n] + pr[n];
            const float a0 = rbf(bf_lo(qv[n].x) * bf_lo(yv[n].x)), a1 = rbf(bf_hi(qv[n].x) * bf_hi(yv[n].x));
            const float a2 = rbf(bf_lo(qv[n].y) * bf_lo(yv[n].y)), a3 = rbf(bf_hi(qv[n].y) * bf_hi(yv[n].y));
            u32x2 o;
            o.x = cvt_pk_bf16(rbf(g * a0) + rbf(omg * rbf(b[0])), rbf(g * a1) + rbf(omg * rbf(b[1])));
            o.y = cvt_pk_bf16(rbf(g * a2) + rbf(omg * rbf(b[2])), rbf(g * a3) + rbf(omg * rbf(b[3])));
            *(u32x2*)(out + (size_t)s * D_ + dcol + 16 * n) = o;
        }
    }
#undef SLOT
#undef STAGE2
#undef LDF
#undef MM2
#undef WAIT_F
}

extern "C" void kernel_launch(void* const* d_in, const int* in_sizes, int n_in, void* d_out, int out_size, void* d_ws, size_t ws_size,
                              hipStream_t stream) {
    const u16* q = (const u16*)d_in[0];
    const u16* k = (const u16*)d_in[1];
    const u16* v = (const u16*)d_in[2];
    const u16* shk = (const u16*)d_in[3];
    const u16* ssmk = (const u16*)d_in[4];
    const u16* ssmD = (const u16*)d_in[5];
    const u16* wg = (const u16*)d_in[7];
    const u16* wu = (const u16*)d_in[8];
    const u16* wd = (const u16*)d_in[9];
    const u16* gw = (const u16*)d_in[10];
    (void)hipFuncSetAttribute((const void*)k1_gemm1_prep, hipFuncAttributeMaxDynamicSharedMemorySize, K1_LDS);
    (void)hipFuncSetAttribute((const void*)k2_conv_gemm2, hipFuncAttributeMaxDynamicSharedMemorySize, K2_LDS);
    k1_gemm1_prep<<<256, 512, K1_LDS, stream>>>(q, k, v, shk, wg, wu, gw, (unsigned char*)d_ws);
    k2_conv_gemm2<<<256, 512, K2_LDS, stream>>>(q, ssmk, ssmD, wd, (unsigned char*)d_ws, (u16*)d_out);
}
```

```cpp
#include <hip/hip_runtime.h>
#include <hip/hip_bf16.h>
#include <type_traits>

#define LAS __attribute__((address_space(3)))
#define GAS __attribute__((address_space(1)))
typedef unsigned short u16;
typedef short bf16x8 __attribute__((ext_vector_type(8)));
typedef short bf16x4 __attribute__((ext_vector_type(4)));
typedef float f32x4 __attribute__((ext_vector_type(4)));
typedef float f32x2 __attribute__((ext_vector_type(2)));
typedef unsigned u32x4 __attribute__((ext_vector_type(4)));
typedef unsigned u32x2 __attribute__((ext_vector_type(2)));
typedef GAS unsigned gu32;
typedef GAS unsigned long long gu64;
#define RLX_AGENT __ATOMIC_RELAXED, __HIP_MEMORY_SCOPE_AGENT

constexpr int S_ = 2048, D_ = 1024, F_ = 3584;
constexpr int URP = 2112;
constexpr size_t WS_H = 0;
constexpr size_t WS_UR = WS_H + (size_t)S_ * F_ * 2;
constexpr size_t WS_G = WS_UR + (size_t)D_ * URP * 2;
constexpr size_t WS_Y2 = WS_G + (size_t)S_ * 4;
constexpr size_t WS_X = WS_Y2 + (size_t)256 * S_ * 8;
constexpr int K1_LDS = 131072, K2_LDS = 163840;

__device__ __forceinline__ unsigned cvt_pk_bf16(float lo, float hi) {
    unsigned r; asm("v_cvt_pk_bf16_f32 %0, %1, %2" : "=v"(r) : "v"(lo), "v"(hi)); return r;
}
__device__ __forceinline__ float bf_lo(unsigned w) { return __uint_as_float(w << 16); }
__device__ __forceinline__ float bf_hi(unsigned w) { return __uint_as_float(w & 0xffff0000u); }
__device__ __forceinline__ float rbf(float x) { return bf_lo(cvt_pk_bf16(x, x)); }

#define GLDS16(gptr, ldsoff) __builtin_amdgcn_global_load_lds((const unsigned*)(gptr), (LAS unsigned*)(lds + (ldsoff)), 16, 0, 0)
#define LDS_B128(off) (*(const LAS bf16x8*)(lds + (off)))
#define LDS_TR(dst, vaddr, imm) asm volatile("ds_read_b64_tr_b16 %0, %1 offset:%2" : "=v"(dst) : "v"(vaddr), "i"(imm) : "memory")
#define CAT8(a, b) __builtin_shufflevector(a, b, 0, 1, 2, 3, 4, 5, 6, 7)
#define WAIT_V(n) asm volatile("s_waitcnt vmcnt(" #n ")" ::: "memory")
#define WAIT_L(n) asm volatile("s_waitcnt lgkmcnt(" #n ")" ::: "memory")
#define BAR() do { asm volatile("" ::: "memory"); __builtin_amdgcn_s_barrier(); asm volatile("" ::: "memory"); } while (0)
#define SCHED() __builtin_amdgcn_sched_barrier(0)

#define STAMP_DECL
#define STAMP()
#define STAMP_OUT(kidx)
__device__ __forceinline__ float gelu_mul(float g, float u) {
    g = rbf(g); u = rbf(u);
    const float p = g * g * (-0.044715f * 2.3022082f) + (-2.3022082f);
    const float e = __builtin_amdgcn_exp2f(g * p);
    const float ge = rbf(g * __builtin_amdgcn_rcpf(1.0f + e));
    return ge * u;
}

__device__ __forceinline__ void fill_handoff(unsigned char* ws) {
    unsigned char* p = ws + WS_Y2 + (size_t)blockIdx.x * 49152 + threadIdx.x * 16;
#pragma unroll
    for (int i = 0; i < 6; ++i) *(u32x4*)(p + i * 8192) = (u32x4){~0u, ~0u, ~0u, ~0u};
}

__device__ __forceinline__ void prep_block(LAS unsigned char* lds, int j, const u16* x, const u16* kk, const u16* vv,
                                           const u16* shk, const u16* gw, unsigned char* ws) {
    const int tid = threadIdx.x, wid = tid >> 6, lane = tid & 63;
    fill_handoff(ws);
    if (tid >= 256) { const int t = tid - 256; *(u32x4*)(ws + WS_UR + ((size_t)(j * 32 + (t >> 3)) * URP + 2048) * 2 + (t & 7) * 16) = (u32x4){0u, 0u, 0u, 0u}; }
    const int cg = tid & 127, rg = tid >> 7;
    float w[8][4];
    {
        const u32x4* wp = (const u32x4*)(shk + cg * 32);
#pragma unroll
        for (int i = 0; i < 4; ++i) { const u32x4 t = wp[i];
            w[2 * i][0] = bf_lo(t.x); w[2 * i][1] = bf_hi(t.x); w[2 * i][2] = bf_lo(t.y); w[2 * i][3] = bf_hi(t.y);
            w[2 * i + 1][0] = bf_lo(t.z); w[2 * i + 1][1] = bf_hi(t.z); w[2 * i + 1][2] = bf_lo(t.w); w[2 * i + 1][3] = bf_hi(t.w); }
    }
#pragma unroll
    for (int hh = 0; hh < 2; ++hh) {
        const int s0 = 64 * j + 16 * rg + 8 * hh;
        u32x4 kr[11], vr[8];
#pragma unroll
        for (int i = 0; i < 11; ++i) { const int s = s0 - 3 + i; kr[i] = (s >= 0) ? *(const u32x4*)(kk + (size_t)s * D_ + cg * 8) : (u32x4){0u, 0u, 0u, 0u}; }
#pragma unroll
        for (int i = 0; i < 8; ++i) vr[i] = *(const u32x4*)(vv + (size_t)(s0 + i) * D_ + cg * 8);
#pragma unroll
        for (int c = 0; c < 8; ++c) {
            float kf[11], o[8];
#pragma unroll
            for (int i = 0; i < 11; ++i) { const unsigned q = kr[i][c >> 1]; kf[i] = (c & 1) ? bf_hi(q) : bf_lo(q); }
#pragma unroll
            for (int i = 0; i < 8; ++i) { const unsigned q = vr[i][c >> 1]; const float vf = (c & 1) ? bf_hi(q) : bf_lo(q);
                float a = kf[i] * w[c][3]; a = fmaf(kf[i + 1], w[c][2], a); a = fmaf(kf[i + 2], w[c][1], a); a = fmaf(kf[i + 3], w[c][0], a);
                o[i] = rbf(a) * vf; }
            u32x4 pk; pk.x = cvt_pk_bf16(o[7], o[6]); pk.y = cvt_pk_bf16(o[5], o[4]); pk.z = cvt_pk_bf16(o[3], o[2]); pk.w = cvt_pk_bf16(o[1], o[0]);
            const int chunk = 7 - (2 * rg + hh);
            *(LAS u32x4*)(lds + (cg * 8 + c) * 128 + ((chunk ^ (cg & 7)) * 16)) = pk;
        }
    }
    {
        const u32x4 g0 = *(const u32x4*)(gw + lane * 8), g1 = *(const u32x4*)(gw + 512 + lane * 8);
#pragma unroll
        for (int r = 0; r < 8; ++r) {
            const int s = 64 * j + wid * 8 + r;
            const u32x4 a0 = *(const u32x4*)(x + (size_t)s * D_ + lane * 8), a1 = *(const u32x4*)(x + (size_t)s * D_ + 512 + lane * 8);
            float acc = 0.f;
#pragma unroll
            for (int i = 0; i < 4; ++i) {
                acc += rbf(bf_lo(a0[i]) * bf_lo(g0[i])); acc += rbf(bf_hi(a0[i]) * bf_hi(g0[i]));
                acc += rbf(bf_lo(a1[i]) * bf_lo(g1[i])); acc += rbf(bf_hi(a1[i]) * bf_hi(g1[i]));
            }
#pragma unroll
            for (int o = 32; o >= 1; o >>= 1) acc += __shfl_xor(acc, o);
            if (lane == 0) { const float z = rbf(acc); ((float*)(ws + WS_G))[s] = rbf(1.0f / (1.0f + __expf(-z))); }
        }
    }
    __syncthreads();
    const size_t xb = (size_t)(1984 - 64 * j) * 2;
#pragma unroll 4
    for (int it = 0; it < 16; ++it) {
        const int ch = it * 64 + (tid >> 3), c = tid & 7;
        const u32x4 t = *(const LAS u32x4*)(lds + ch * 128 + ((c ^ ((ch >> 3) & 7)) * 16));
        *(u32x4*)(ws + WS_UR + (size_t)ch * URP * 2 + xb + c * 16) = t;
    }
}

__global__ __launch_bounds__(512, 2) void k1_gemm1_prep(const u16* __restrict__ x, const u16* __restrict__ kk, const u16* __restrict__ vv,
                                                        const u16* __restrict__ shk, const u16* __restrict__ wg, const u16* __restrict__ wu,
                                                        const u16* __restrict__ gw, const u16* __restrict__ ssmk, const u16* __restrict__ wd,
                                                        unsigned char* __restrict__ ws) {
    extern __shared__ __attribute__((aligned(16))) unsigned char smem[];
    LAS unsigned char* lds = (LAS unsigned char*)smem;
    const int xg = blockIdx.x & 7, l = blockIdx.x >> 3;
    STAMP_DECL; STAMP();
    if (l >= 28) { prep_block(lds, xg * 4 + (l - 28), x, kk, vv, shk, gw, ws); WAIT_V(0); STAMP(); STAMP_OUT(0); return; }
    const int pm = 4 * (xg & 1) + (l & 3), pn = 7 * (xg >> 1) + (l >> 2);
    const int tid = threadIdx.x, wid = __builtin_amdgcn_readfirstlane(tid >> 6), lane = tid & 63, wr = wid >> 2, wc = wid & 3, fr = lane & 15, fq = lane >> 4;
    constexpr int HTB = 16384;
    unsigned voffA[2], voffB[2];
#pragma unroll
    for (int i = 0; i < 2; ++i) {
        const int piece = wid + 8 * i;
        { const int row = piece * 8 + (lane >> 3), ch = (lane & 7) ^ (lane >> 3); voffA[i] = (unsigned)(row * 2048 + ch * 16); }
        { const int kr = piece * 4 + (lane >> 4), fs = (kr & 3) | (((kr >> 3) & 1) << 2), ch = (lane & 15) ^ (2 * fs); voffB[i] = (unsigned)(kr * (F_ * 2) + ch * 16); }
    }
    const unsigned ldsw = (unsigned)wid * 1024u;
    const int aoff0 = ((wr * 64 + fr) >> 3) * 1024 + (fr & 7) * 128 + ((fq ^ (fr & 7)) * 16), aoff1 = aoff0 ^ 64;
    const int tq = (lane & 15) >> 2, tp = lane & 3;
    const unsigned boff0 = (unsigned)(size_t)lds + 4 * HTB + (8 * fq + tq) * 256 + (((4 * wc + (tp >> 1)) ^ (2 * (tq | ((fq & 1) << 2)))) * 16) + 8 * (tp & 1), boff1 = boff0 ^ 32;
    const char* cA = (const char*)x + (size_t)pm * 256 * 2048;
    const char* cG = (const char*)wg + (size_t)pn * 256;
    const char* cU = (const char*)wu + (size_t)pn * 256;
    constexpr size_t kA = 128, kB = (size_t)64 * F_ * 2, hA = (size_t)128 * 2048;
#define SA(b, h) (((b) * 2 + (h)) * HTB)
#define SB(b, h) ((4 + (b) * 2 + (h)) * HTB)
#define STAGE(bufoff, gbase, voff) do { _Pragma("unroll") for (int _i = 0; _i < 2; ++_i) GLDS16((gbase) + (voff)[_i], (bufoff) + ldsw + _i * 8192); } while (0)
#define LDA(dst, b, h) do { _Pragma("unroll") for (int m = 0; m < 4; ++m) { dst[m][0] = LDS_B128(SA(b, h) + aoff0 + m * 2048); dst[m][1] = LDS_B128(SA(b, h) + aoff1 + m * 2048); } } while (0)
#define LDB(dst, b, h) do { \
        LDS_TR(dst[0], boff0, ((b) * 2 + (h)) * HTB); LDS_TR(dst[1], boff0, ((b) * 2 + (h)) * HTB + 1024); \
        LDS_TR(dst[2], boff0, ((b) * 2 + (h)) * HTB + 8192); LDS_TR(dst[3], boff0, ((b) * 2 + (h)) * HTB + 9216); \
        LDS_TR(dst[4], boff1, ((b) * 2 + (h)) * HTB); LDS_TR(dst[5], boff1, ((b) * 2 + (h)) * HTB + 1024); \
        LDS_TR(dst[6], boff1, ((b) * 2 + (h)) * HTB + 8192); LDS_TR(dst[7], boff1, ((b) * 2 + (h)) * HTB + 9216); } while (0)
#define WAIT_B(B) asm volatile("s_waitcnt lgkmcnt(0)" : "+v"(B[0]), "+v"(B[1]), "+v"(B[2]), "+v"(B[3]), "+v"(B[4]), "+v"(B[5]), "+v"(B[6]), "+v"(B[7]) :: "memory")
#define MMA(ai, bj, At, Bt) do { __builtin_amdgcn_s_setprio(1); _Pragma("unroll") for (int m = 0; m < 4; ++m) _Pragma("unroll") for (int n = 0; n < 2; ++n) _Pragma("unroll") for (int k = 0; k < 2; ++k) \
        acc[ai][bj][m][n] = __builtin_amdgcn_mfma_f32_16x16x32_bf16(CAT8(Bt[4 * n + 2 * k], Bt[4 * n + 2 * k + 1]), At[m][k], acc[ai][bj][m][n], 0, 0, 0); __builtin_amdgcn_s_setprio(0); } while (0)
    f32x4 acc[2][2][4][2];
#pragma unroll
    for (int a = 0; a < 2; ++a)
#pragma unroll
        for (int b = 0; b < 2; ++b)
#pragma unroll
            for (int m = 0; m < 4; ++m)
#pragma unroll
                for (int n = 0; n < 2; ++n) acc[a][b][m][n] = (f32x4){0.f, 0.f, 0.f, 0.f};
    bf16x8 At[4][2]; bf16x4 B0[8], B1[8];
    constexpr int nt = D_ / 64;
    STAGE(SB(0, 0), cG, voffB); STAGE(SA(0, 0), cA, voffA); STAGE(SB(0, 1), cU, voffB); STAGE(SA(0, 1), cA + hA, voffA);
    if (wr == 1) BAR();
    WAIT_V(4); BAR();
    STAGE(SB(1, 0), cG + kB, voffB); STAGE(SA(1, 0), cA + kA, voffA); STAGE(SB(1, 1), cU + kB, voffB);
    WAIT_V(6); BAR();
    fill_handoff(ws);
    STAMP();
    const char* pA = (const char*)ssmk + (size_t)pm * 256 * 2048 + (size_t)(pn & 15) * kA;
    const char* pB = (const char*)wd + (size_t)pn * 256 + (size_t)(2 * pm) * kB;
    for (int t = 0; t < nt; t += 2) {
        const bool last = (t + 2 >= nt);
        const char* a1 = cA + (size_t)(t + 1) * kA;
        const char* a2 = last ? pA : cA + (size_t)(t + 2) * kA; const char* g2 = last ? pB : cG + (size_t)(t + 2) * kB; const char* u2 = last ? pB + kB : cU + (size_t)(t + 2) * kB;
        const char* a3 = last ? cA : a2 + kA; const char* g3 = last ? cG : g2 + kB; const char* u3 = last ? cU : u2 + kB;
        LDB(B0, 0, 0); SCHED(); LDA(At, 0, 0); STAGE(SA(1, 1), a1 + hA, voffA);
        WAIT_L(8); BAR(); WAIT_B(B0); MMA(0, 0, At, B0); BAR(); SCHED();
        LDB(B1, 0, 1); STAGE(SB(0, 0), g2, voffB);
        BAR(); WAIT_B(B1); MMA(0, 1, At, B1); BAR();
        LDA(At, 0, 1); STAGE(SA(0, 0), a2, voffA);
        BAR(); WAIT_L(0); MMA(1, 0, At, B0); BAR(); SCHED();
        STAGE(SB(0, 1), u2, voffB);
        WAIT_V(6); BAR(); MMA(1, 1, At, B1); BAR();
        LDB(B0, 1, 0); SCHED(); LDA(At, 1, 0); STAGE(SA(0, 1), a2 + hA, voffA);
        WAIT_L(8); BAR(); WAIT_B(B0); MMA(0, 0, At, B0); BAR(); SCHED();
        LDB(B1, 1, 1); STAGE(SB(1, 0), g3, voffB);
        BAR(); WAIT_B(B1); MMA(0, 1, At, B1); BAR();
        LDA(At, 1, 1); STAGE(SA(1, 0), a3, voffA);
        BAR(); WAIT_L(0); MMA(1, 0, At, B0); BAR(); SCHED();
        STAGE(SB(1, 1), u3, voffB);
        WAIT_V(6); BAR(); MMA(1, 1, At, B1); BAR();
    }
    STAMP();
    {
        unsigned char* hp = ws + WS_H + ((size_t)(pm * 16 + wr * 4) * 112 + pn * 4 + wc) * 1024 + lane * 16;
#pragma unroll
        for (int ai = 0; ai < 2; ++ai)
#pragma unroll
            for (int m = 0; m < 4; ++m) {
                const f32x4 g0 = acc[ai][0][m][0], g1 = acc[ai][0][m][1], u0 = acc[ai][1][m][0], u1 = acc[ai][1][m][1];
                u32x4 o;
                o.x = cvt_pk_bf16(gelu_mul(g0[0], u0[0]), gelu_mul(g0[1], u0[1])); o.y = cvt_pk_bf16(gelu_mul(g0[2], u0[2]), gelu_mul(g0[3], u0[3]));
                o.z = cvt_pk_bf16(gelu_mul(g1[0], u1[0]), gelu_mul(g1[1], u1[1])); o.w = cvt_pk_bf16(gelu_mul(g1[2], u1[2]), gelu_mul(g1[3], u1[3]));
                *(u32x4*)(hp + (size_t)(ai * 8 + m) * 112 * 1024) = o;
            }
    }
    STAMP();
    WAIT_V(0);
    STAMP(); STAMP_OUT(0);
    if (wr == 0) BAR();
#undef SA
#undef SB
#undef STAGE
#undef LDA
#undef LDB
#undef MMA
#undef WAIT_B
}

constexpr int CV_UP = 4160, CV_HP = 4640, CV_HB = 4 * CV_UP, CV_CH = CV_HB + 2 * CV_HP;
constexpr int RB = 106496;
constexpr int NT2 = 28;
constexpr int CV0 = 3;
constexpr int CVX = CV0 + 12, CVM = CVX + 1, CVS = CVM + 1;
constexpr unsigned kSpinCap = 1u << 20;
constexpr int vm_b(int t) { return t + 3 < NT2 ? 2 : 0; }
constexpr int vm_s(int t) { return t == CVS ? 2 : 0; }
constexpr int vm_a(int t) { return t + 4 < NT2 ? 2 : 0; }
constexpr int vm_all(int t) { return vm_b(t) + vm_s(t) + vm_a(t); }
constexpr int vm_wait(int t) {
    return t == 0 ? 8 + 12 : t == 1 ? 4 + 12 + vm_all(0)
         : t + 1 < NT2 ? vm_s(t - 2) + vm_a(t - 2) + vm_all(t - 1) : 0;
}
template <int N> __device__ __forceinline__ void wait_vm() { asm volatile("s_waitcnt vmcnt(%0)" :: "n"(N) : "memory"); }
__device__ constexpr int kSlA[3][12] = {{0,0,0,0,1,1,1,1,2,2,2,2}, {0,0,0,0,1,1,1,2,2,3,3,3}, {3,3,4,4,4,4,5,5,5,6,6,7}};
__device__ constexpr int kSlB[3][12] = {{0,1,2,3,0,1,2,3,0,1,2,3}, {4,5,6,7,4,5,6,4,5,4,3,2}, {0,1,0,1,2,3,0,1,2,0,1,0}};
constexpr int kUlo[3] = {0, 0, 3}, kUhi[3] = {2, 3, 7}, kHlo[3] = {0, 2, 0}, kHhi[3] = {3, 7, 3};

struct K2State {
    f32x4 acc[2][8];
    bf16x8 A[4][2];
    bf16x4 B[16];
    f32x4 cacc[8];
};

__global__ __launch_bounds__(512, 2) void k2_conv_gemm2(const u16* __restrict__ q, const u16* __restrict__ ssmk, const u16* __restrict__ ssmD,
                                                        const u16* __restrict__ wd, unsigned char* __restrict__ ws, u16* __restrict__ out) {
    extern __shared__ __attribute__((aligned(16))) unsigned char smem[];
    LAS unsigned char* lds = (LAS unsigned char*)smem;
    const unsigned lbase = (unsigned)(size_t)lds;
    const int tid = threadIdx.x, wid = __builtin_amdgcn_readfirstlane(tid >> 6), lane = tid & 63, fr = lane & 15, fq = lane >> 4;
    const int bid = blockIdx.x, xg = bid & 7, l = bid >> 3;
    const int z = xg & 1, tm = 8 * ((xg >> 1) & 1) + (l & 7), tn = 4 * (xg >> 2) + (l >> 3);
    const int kh = wid >> 2, wm = wid & 3;
    STAMP_DECL; STAMP();
    unsigned voffB[2];
#pragma unroll
    for (int i = 0; i < 2; ++i) { const int kr = (wid + 8 * i) * 4 + (lane >> 4), ch = (lane & 15) ^ (2 * (kr & 7)); voffB[i] = (unsigned)(kr * 2048 + ch * 16); }
    const unsigned ldsw = (unsigned)wid * 1024u, voffA = (unsigned)lane * 16u;
    const char* cA0 = (const char*)(ws + WS_H) + ((size_t)(tm * 8 + wm * 2) * 112 + z * 56 + kh) * 1024;
    const char* cA1 = cA0 + 112 * 1024;
    const char* cB = (const char*)wd + (size_t)z * 1792 * 2048 + (size_t)tn * 256;
    constexpr size_t kB = (size_t)64 * 2048;
    K2State st;
#define STAGEB(t) do { const char* _b = cB + (size_t)(t) * kB; GLDS16(_b + voffB[0], RB + ((t) % 3) * 16384 + ldsw); GLDS16(_b + voffB[1], RB + ((t) % 3) * 16384 + ldsw + 8192); } while (0)
#define LOADA(t) do { asm volatile("global_load_dwordx4 %0, %1, %2" : "=v"(st.A[(t) & 3][0]) : "v"(voffA), "s"(cA0 + (size_t)(t) * 2048) : "memory"); \
        asm volatile("global_load_dwordx4 %0, %1, %2" : "=v"(st.A[(t) & 3][1]) : "v"(voffA), "s"(cA1 + (size_t)(t) * 2048) : "memory"); } while (0)
    const int ch = wid & 3, hf = wid >> 2, d = bid * 4 + ch, CB = ch * CV_CH;
    const unsigned dpair = ((const unsigned*)ssmD)[d >> 1];
    const float dval = (d & 1) ? bf_hi(dpair) : bf_lo(dpair);
    const u32x2 h03 = *(const u32x2*)(ssmk + (size_t)d * S_);
    STAGEB(0); LOADA(0); STAGEB(1); LOADA(1); STAGEB(2); LOADA(2); LOADA(3);
    {
        const char* ur = (const char*)(ws + WS_UR) + (size_t)d * URP * 2;
        const auto hrs = __builtin_amdgcn_make_buffer_rsrc((void*)ssmk, 0, D_ * S_ * 2, 0x00020000);
#pragma unroll
        for (int c2 = 0; c2 < 2; ++c2) { const int c = 2 * hf + c2;
#pragma unroll
            for (int i = 0; i < 4; ++i) GLDS16(ur + (3 - c) * 2 + i * 1024 + lane * 16, CB + c * CV_UP + i * 1024); }
#pragma unroll
        for (int i = 0; i < 4; ++i)
            __builtin_amdgcn_raw_ptr_buffer_load_lds(hrs, (LAS void*)(lds + CB + CV_HB + hf * CV_HP + (29 - hf) * 16 + i * 1024), 16,
                                                     d * (S_ * 2) + (1 - hf) * 8 + i * 1024 + lane * 16, 0, 0, 0);
        if (lane < 28) *(LAS u32x4*)(lds + CB + CV_HB + hf * CV_HP + lane * 16) = (u32x4){0u, 0u, 0u, 0u};
        else if (lane < 34) { const int e = lane - 28; *(LAS u32x4*)(lds + CB + (2 * hf + e / 3) * CV_UP + 4096 + (e % 3) * 16) = (u32x4){0u, 0u, 0u, 0u}; }
        else if (lane == 34 && hf == 0) *(LAS u32x4*)(lds + CB + CV_HB + 28 * 16) = (u32x4){0u, 0u, h03.x, h03.y};
    }
    const int tq = fr >> 2, tp4 = lane & 3;
    unsigned boff[8];
    { const unsigned b0 = lbase + RB + (32 * kh + 4 * fq + tq) * 256 + 8 * (tp4 & 1) + (((tp4 >> 1) ^ (2 * ((4 * fq + tq) & 7))) * 16);
#pragma unroll
      for (int n = 0; n < 8; ++n) boff[n] = b0 ^ (32 * n); }
    const unsigned ua = lbase + CB + (fr & 3) * CV_UP + 2 * (2040 - 8 * (fr >> 2) + 8 * fq) - 64 * 59 - 256 * hf;
    const unsigned ha = lbase + CB + CV_HB + (fr & 1) * CV_HP + 2 * (224 + 32 * (fr >> 1) + 8 * fq) - 192 - 256 * hf;
#pragma unroll
    for (int m = 0; m < 2; ++m)
#pragma unroll
        for (int n = 0; n < 8; ++n) st.acc[m][n] = (f32x4){0.f, 0.f, 0.f, 0.f};
#pragma unroll
    for (int n = 0; n < 8; ++n) st.cacc[n] = (f32x4){0.f, 0.f, 0.f, 0.f};
#define RDB(t, n) do { LDS_TR(st.B[2 * (n)], boff[n], ((t) % 3) * 16384); LDS_TR(st.B[2 * (n) + 1], boff[n], ((t) % 3) * 16384 + 4096); } while (0)
#define RD128(dst, vaddr, imm) asm volatile("ds_read_b128 %0, %1 offset:%2" : "=v"(dst) : "v"(vaddr), "i"(imm) : "memory")
    wait_vm<12 + 12>(); WAIT_L(0); BAR();
    STAMP();
#pragma unroll
    for (int n = 0; n < 8; ++n) RDB(0, n);
    const auto yrs = __builtin_amdgcn_make_buffer_rsrc((void*)(ws + WS_Y2 + (size_t)bid * 16384), 0, 16384, 0x00020000);

    auto trip = [&](auto tc) __attribute__((always_inline)) {
        constexpr int t = decltype(tc)::value;
        wait_vm<vm_wait(t)>();
        asm volatile("s_waitcnt lgkmcnt(0)" : "+v"(st.B[0]), "+v"(st.B[1]), "+v"(st.B[2]), "+v"(st.B[3]), "+v"(st.B[4]), "+v"(st.B[5]), "+v"(st.B[6]), "+v"(st.B[7]),
                     "+v"(st.B[8]), "+v"(st.B[9]), "+v"(st.B[10]), "+v"(st.B[11]), "+v"(st.B[12]), "+v"(st.B[13]), "+v"(st.B[14]), "+v"(st.B[15]),
                     "+v"(st.A[t & 3][0]), "+v"(st.A[t & 3][1]) :: "memory");
        BAR();
        constexpr bool cv = (t >= CV0 && t < CVX);
        constexpr int cc = cv ? (t - CV0) / 3 : 0, sl = cv ? (t - CV0) % 3 : 0;
        bf16x8 U[8], Hf[8];
        if constexpr (cv) {
#pragma unroll
            for (int a = kUlo[sl]; a <= kUhi[sl]; ++a) RD128(U[a], ua, 64 * (59 - 8 * a - cc));
#pragma unroll
            for (int b = kHlo[sl]; b <= kHhi[sl]; ++b) RD128(Hf[b], ha, 64 * (8 * b - cc + 3));
        }
        if constexpr (t == CVX) {
#pragma unroll
            for (int i = 0; i < 4; ++i) *(LAS f32x4*)(lds + CB + CV_HB + hf * CV_HP + lane * 16 + i * 1024) = hf ? st.cacc[i] : st.cacc[4 + i];
        }
        if constexpr (t == CVM) {
#pragma unroll
            for (int i = 0; i < 4; ++i) {
                const f32x4 y = (hf ? st.cacc[4 + i] : st.cacc[i]) + *(const LAS f32x4*)(lds + CB + CV_HB + (1 - hf) * CV_HP + lane * 16 + i * 1024);
                const int s0 = 256 * (4 * hf + i) + 32 * (fr >> 1) + 4 * (fr & 1) + 8 * fq;
                const u32x2 kv = *(const LAS u32x2*)(lds + CB + 3 * CV_UP + 2 * (2044 - s0));
                const float k3 = bf_lo(kv.x), k2 = bf_hi(kv.x), k1 = bf_lo(kv.y), k0 = bf_hi(kv.y);
                const unsigned p01 = cvt_pk_bf16(rbf(y[0]) + rbf(k0 * dval), rbf(y[1]) + rbf(k1 * dval));
                const unsigned p23 = cvt_pk_bf16(rbf(y[2]) + rbf(k2 * dval), rbf(y[3]) + rbf(k3 * dval));
                LAS u16* tp = (LAS u16*)(lds + (2 * hf + (i >> 1)) * CV_CH + (s0 & 511) * 8 + ((s0 & 511) >> 5) * 8 + ch * 2);
                tp[0] = (u16)p01; tp[4] = (u16)(p01 >> 16); tp[8] = (u16)p23; tp[12] = (u16)(p23 >> 16);
            }
        }
        u32x2 yo[4];
        if constexpr (t == CVS) {
#pragma unroll
            for (int i = 0; i < 2; ++i) { const int s = (i * 512 + tid) * 2; const int o = (s >> 9) * CV_CH + (s & 511) * 8 + ((s & 511) >> 5) * 8;
                yo[2 * i] = *(const LAS u32x2*)(lds + o); yo[2 * i + 1] = *(const LAS u32x2*)(lds + o + 8); }
        }
        if constexpr (vm_b(t) != 0) STAGEB(t + 3);
        SCHED();
#define GRP(j) do { _Pragma("unroll") for (int n = 2 * (j); n < 2 * (j) + 2; ++n) _Pragma("unroll") for (int m = 0; m < 2; ++m) \
            st.acc[m][n] = __builtin_amdgcn_mfma_f32_16x16x32_bf16(CAT8(st.B[2 * n], st.B[2 * n + 1]), st.A[t & 3][m], st.acc[m][n], 0, 0, 0); \
        SCHED(); if constexpr (t + 1 < NT2) { RDB(t + 1, 2 * (j)); RDB(t + 1, 2 * (j) + 1); } SCHED(); } while (0)
        GRP(0); GRP(1);
        if constexpr (cv) {
            if constexpr (sl == 0) asm volatile("s_waitcnt lgkmcnt(8)" : "+v"(U[0]), "+v"(U[1]), "+v"(U[2]), "+v"(Hf[0]), "+v"(Hf[1]), "+v"(Hf[2]), "+v"(Hf[3]) :: "memory");
            if constexpr (sl == 1) asm volatile("s_waitcnt lgkmcnt(8)" : "+v"(U[0]), "+v"(U[1]), "+v"(U[2]), "+v"(U[3]), "+v"(Hf[2]), "+v"(Hf[3]), "+v"(Hf[4]), "+v"(Hf[5]), "+v"(Hf[6]), "+v"(Hf[7]) :: "memory");
            if constexpr (sl == 2) asm volatile("s_waitcnt lgkmcnt(8)" : "+v"(U[3]), "+v"(U[4]), "+v"(U[5]), "+v"(U[6]), "+v"(U[7]), "+v"(Hf[0]), "+v"(Hf[1]), "+v"(Hf[2]), "+v"(Hf[3]) :: "memory");
#pragma unroll
            for (int i = 0; i < 12; ++i)
                st.cacc[kSlA[sl][i] + kSlB[sl][i]] = __builtin_amdgcn_mfma_f32_16x16x32_bf16(U[kSlA[sl][i]], Hf[kSlB[sl][i]], st.cacc[kSlA[sl][i] + kSlB[sl][i]], 0, 0, 0);
            SCHED();
        }
        if constexpr (t == CVS) {
#pragma unroll
            for (int i = 0; i < 2; ++i) {
                u32x4 v = {yo[2 * i].x, yo[2 * i].y, yo[2 * i + 1].x, yo[2 * i + 1].y};
#pragma unroll
                for (int e = 0; e < 4; ++e) v[e] = (v[e] == ~0u) ? 0x7fc07fc0u : v[e];
                __builtin_amdgcn_raw_buffer_store_b128(v, yrs, (i * 512 + tid) * 16, 0, 16);
            }
            SCHED();
        }
        GRP(2); GRP(3);
        if constexpr (vm_a(t) != 0) LOADA(t + 4);
        SCHED();
#undef GRP
    };
#define TRIP4(t) trip(std::integral_constant<int, (t)>{}); trip(std::integral_constant<int, (t) + 1>{}); trip(std::integral_constant<int, (t) + 2>{}); trip(std::integral_constant<int, (t) + 3>{})
    TRIP4(0); TRIP4(4); TRIP4(8); TRIP4(12); TRIP4(16); TRIP4(20); TRIP4(24);
    STAMP();
    {
        const int s = tm * 128 + wm * 32 + z * 16 + fr, dcol = tn * 128 + kh * 64 + 4 * fq;
        u32x2 qv[4];
#pragma unroll
        for (int n = 0; n < 4; ++n) qv[n] = *(const u32x2*)(q + (size_t)s * D_ + dcol + 16 * n);
        const float g = ((const float*)(ws + WS_G))[s];
        const int xw = wid * 8192 + lane * 16, xr = (wid ^ 4) * 8192 + lane * 16;
#pragma unroll
        for (int n = 0; n < 4; ++n) {
            *(LAS f32x4*)(lds + xw + n * 1024) = z ? (kh ? st.acc[0][n] : st.acc[0][4 + n]) : (kh ? st.acc[1][n] : st.acc[1][4 + n]);
            *(LAS f32x4*)(lds + xw + (4 + n) * 1024) = z ? (kh ? st.acc[1][n] : st.acc[1][4 + n]) : (kh ? st.acc[0][n] : st.acc[0][4 + n]);
        }
        WAIT_L(0); BAR();
        const auto xs = __builtin_amdgcn_make_buffer_rsrc((void*)(ws + WS_X + (size_t)bid * 32768), 0, 32768, 0x00020000);
#pragma unroll
        for (int n = 0; n < 4; ++n) {
            const f32x4 own = z ? (kh ? st.acc[0][4 + n] : st.acc[0][n]) : (kh ? st.acc[1][4 + n] : st.acc[1][n]);
            u32x4 v = __builtin_bit_cast(u32x4, own + *(const LAS f32x4*)(lds + xr + n * 1024));
#pragma unroll
            for (int e = 0; e < 4; ++e) v[e] = (v[e] == ~0u) ? 0x7fc00000u : v[e];
            __builtin_amdgcn_raw_buffer_store_b128(v, xs, wid * 4096 + n * 1024 + lane * 16, 0, 16);
        }
        f32x4 keep[4];
#pragma unroll
        for (int n = 0; n < 4; ++n)
            keep[n] = (z ? (kh ? st.acc[1][4 + n] : st.acc[1][n]) : (kh ? st.acc[0][4 + n] : st.acc[0][n])) + *(const LAS f32x4*)(lds + xr + (4 + n) * 1024);
        STAMP();
        const auto xp = __builtin_amdgcn_make_buffer_rsrc((void*)(ws + WS_X + (size_t)(bid ^ 1) * 32768), 0, 32768, 0x00020000);
        const auto y2 = __builtin_amdgcn_make_buffer_rsrc((void*)(ws + WS_Y2), 0, 256 * S_ * 8, 0x00020000);
        u32x4 pr[4]; u32x2 yv[4];
        for (unsigned spins = 0; spins < kSpinCap; ++spins) {
            bool ok = true;
#pragma unroll
            for (int n = 0; n < 4; ++n) {
                pr[n] = __builtin_amdgcn_raw_buffer_load_b128(xp, wid * 4096 + n * 1024 + lane * 16, 0, 16);
                yv[n] = __builtin_amdgcn_raw_buffer_load_b64(y2, (((dcol >> 2) + 4 * n) * S_ + s) * 8, 0, 16);
            }
#pragma unroll
            for (int n = 0; n < 4; ++n)
                ok = ok && pr[n].x != ~0u && pr[n].y != ~0u && pr[n].z != ~0u && pr[n].w != ~0u && yv[n].x != ~0u && yv[n].y != ~0u;
            if (__all(ok)) break;
            asm volatile("s_sleep 1" ::: "memory");
        }
        STAMP();
        const float omg = rbf(1.0f - g);
#pragma unroll
        for (int n = 0; n < 4; ++n) {
            const f32x4 b = keep[n] + __builtin_bit_cast(f32x4, pr[n]);
            const float a0 = rbf(bf_lo(qv[n].x) * bf_lo(yv[n].x)), a1 = rbf(bf_hi(qv[n].x) * bf_hi(yv[n].x));
            const float a2 = rbf(bf_lo(qv[n].y) * bf_lo(yv[n].y)), a3 = rbf(bf_hi(qv[n].y) * bf_hi(yv[n].y));
            u32x2 o;
            o.x = cvt_pk_bf16(rbf(g * a0) + rbf(omg * rbf(b[0])), rbf(g * a1) + rbf(omg * rbf(b[1])));
            o.y = cvt_pk_bf16(rbf(g * a2) + rbf(omg * rbf(b[2])), rbf(g * a3) + rbf(omg * rbf(b[3])));
            *(u32x2*)(out + (size_t)s * D_ + dcol + 16 * n) = o;
        }
        STAMP(); WAIT_V(0); STAMP(); STAMP_OUT(1);
    }
#undef STAGEB
#undef LOADA
#undef RDB
#undef RD128
#undef TRIP4
}

extern "C" void kernel_launch(void* const* d_in, const int* in_sizes, int n_in, void* d_out, int out_size, void* d_ws, size_t ws_size,
                              hipStream_t stream) {
    const u16* q = (const u16*)d_in[0];
    const u16* k = (const u16*)d_in[1];
    const u16* v = (const u16*)d_in[2];
    const u16* shk = (const u16*)d_in[3];
    const u16* ssmk = (const u16*)d_in[4];
    const u16* ssmD = (const u16*)d_in[5];
    const u16* wg = (const u16*)d_in[7];
    const u16* wu = (const u16*)d_in[8];
    const u16* wd = (const u16*)d_in[9];
    const u16* gw = (const u16*)d_in[10];
    (void)hipFuncSetAttribute((const void*)k1_gemm1_prep, hipFuncAttributeMaxDynamicSharedMemorySize, K1_LDS);
    (void)hipFuncSetAttribute((const void*)k2_conv_gemm2, hipFuncAttributeMaxDynamicSharedMemorySize, K2_LDS);
    k1_gemm1_prep<<<256, 512, K1_LDS, stream>>>(q, k, v, shk, wg, wu, gw, ssmk, wd, (unsigned char*)d_ws);
    k2_conv_gemm2<<<256, 512, K2_LDS, stream>>>(q, ssmk, ssmD, wd, (unsigned char*)d_ws, (u16*)d_out);
}
```

```cpp
#include <hip/hip_runtime.h>
#include <hip/hip_bf16.h>
#include <type_traits>

#define LAS __attribute__((address_space(3)))
#define GAS __attribute__((address_space(1)))
typedef unsigned short u16;
typedef short bf16x8 __attribute__((ext_vector_type(8)));
typedef short bf16x4 __attribute__((ext_vector_type(4)));
typedef float f32x4 __attribute__((ext_vector_type(4)));
typedef float f32x2 __attribute__((ext_vector_type(2)));
typedef unsigned u32x4 __attribute__((ext_vector_type(4)));
typedef unsigned u32x2 __attribute__((ext_vector_type(2)));
typedef GAS unsigned gu32;
typedef GAS unsigned long long gu64;
#define RLX_AGENT __ATOMIC_RELAXED, __HIP_MEMORY_SCOPE_AGENT

constexpr int S_ = 2048, D_ = 1024, F_ = 3584;
constexpr int URP = 2112;
constexpr size_t WS_H = 0;
constexpr size_t WS_UR = WS_H + (size_t)S_ * F_ * 2;
constexpr size_t WS_G = WS_UR + (size_t)D_ * URP * 2;
constexpr size_t WS_Y2 = WS_G + (size_t)S_ * 4;
constexpr size_t WS_X = WS_Y2 + (size_t)256 * S_ * 8;
constexpr size_t WS_FL = WS_X + (size_t)256 * 32768;
constexpr int K1_LDS = 139264, K2_LDS = 163840;

__device__ __forceinline__ unsigned cvt_pk_bf16(float lo, float hi) {
    unsigned r; asm("v_cvt_pk_bf16_f32 %0, %1, %2" : "=v"(r) : "v"(lo), "v"(hi)); return r;
}
__device__ __forceinline__ float bf_lo(unsigned w) { return __uint_as_float(w << 16); }
__device__ __forceinline__ float bf_hi(unsigned w) { return __uint_as_float(w & 0xffff0000u); }
__device__ __forceinline__ float rbf(float x) { return bf_lo(cvt_pk_bf16(x, x)); }

#define GLDS16(gptr, ldsoff) __builtin_amdgcn_global_load_lds((const unsigned*)(gptr), (LAS unsigned*)(lds + (ldsoff)), 16, 0, 0)
#define LDS_B128(off) (*(const LAS bf16x8*)(lds + (off)))
#define LDS_TR(dst, vaddr, imm) asm volatile("ds_read_b64_tr_b16 %0, %1 offset:%2" : "=v"(dst) : "v"(vaddr), "i"(imm) : "memory")
#define CAT8(a, b) __builtin_shufflevector(a, b, 0, 1, 2, 3, 4, 5, 6, 7)
#define WAIT_V(n) asm volatile("s_waitcnt vmcnt(" #n ")" ::: "memory")
#define WAIT_L(n) asm volatile("s_waitcnt lgkmcnt(" #n ")" ::: "memory")
#define BAR() do { asm volatile("" ::: "memory"); __builtin_amdgcn_s_barrier(); asm volatile("" ::: "memory"); } while (0)
#define SCHED() __builtin_amdgcn_sched_barrier(0)

#define STAMP_DECL
#define STAMP()
#define STAMP_OUT(kidx)
#define SEG_DECL
#define SEG(grp, i)
#define SEG_OUT()
__device__ __forceinline__ float gelu_mul(float g, float u) {
    g = rbf(g); u = rbf(u);
    const float p = g * g * (-0.044715f * 2.3022082f) + (-2.3022082f);
    const float e = __builtin_amdgcn_exp2f(g * p);
    const float ge = rbf(g * __builtin_amdgcn_rcpf(1.0f + e));
    return ge * u;
}

__device__ __forceinline__ void prep_block(LAS unsigned char* lds, int j, const u16* x, const u16* kk, const u16* vv, const u16* shk, const u16* gw,
                                           const u16* wg, const u16* wu, unsigned char* ws) {
    const int tid = threadIdx.x, wid = __builtin_amdgcn_readfirstlane(tid >> 6), lane = tid & 63;
    const unsigned ldsw = (unsigned)wid * 1024u;
    STAMP_DECL; STAMP();
    if (tid < 72) *(unsigned*)(ws + WS_FL + (size_t)(j * 72 + tid) * 64) = 0u;
    if (tid >= 256) { const int t = tid - 256; *(u32x4*)(ws + WS_UR + ((size_t)(j * 32 + (t >> 3)) * URP + 2048) * 2 + (t & 7) * 16) = (u32x4){0u, 0u, 0u, 0u}; }
    {
        const char* w = (const char*)((wid < 4) ? wg : wu) + (size_t)j * 14336 + (wid & 3) * 1024 + lane * 16;
        const char* xs = (const char*)x + (size_t)(64 * j + 8 * wid + (lane >> 3)) * 2048 + (lane & 7) * 16;
#pragma unroll 1
        for (int t = 2; t < 16; ++t) {
            const char* p = w + (size_t)t * 64 * 7168;
            GLDS16(xs + t * 128, ldsw); GLDS16(p, ldsw); GLDS16(p + 4096, ldsw); GLDS16(p + 8192, ldsw);
            if ((wid & 3) < 2) GLDS16(p + 12288, ldsw);
            WAIT_V(24);
        }
    }
    WAIT_V(0);
    __syncthreads();
    STAMP();
    const int cg = tid & 127, rg = tid >> 7;
    {
        const int s0 = 64 * j + 16 * rg;
        u32x4 kr[19], vr[16], wr4[4];
#pragma unroll
        for (int i = 0; i < 4; ++i) wr4[i] = ((const u32x4*)(shk + cg * 32))[i];
#pragma unroll
        for (int i = 0; i < 19; ++i) { const int s = s0 - 3 + i; kr[i] = *(const u32x4*)(kk + (size_t)(s < 0 ? 0 : s) * D_ + cg * 8); }
#pragma unroll
        for (int i = 0; i < 16; ++i) vr[i] = *(const u32x4*)(vv + (size_t)(s0 + i) * D_ + cg * 8);
        if (s0 == 0) kr[0] = kr[1] = kr[2] = (u32x4){0u, 0u, 0u, 0u};
#pragma unroll
        for (int cp = 0; cp < 4; ++cp) {
            const f32x2 w0 = {bf_lo(wr4[cp].x), bf_lo(wr4[cp].z)}, w1 = {bf_hi(wr4[cp].x), bf_hi(wr4[cp].z)};
            const f32x2 w2 = {bf_lo(wr4[cp].y), bf_lo(wr4[cp].w)}, w3 = {bf_hi(wr4[cp].y), bf_hi(wr4[cp].w)};
            f32x2 kf[19], o[16];
#pragma unroll
            for (int i = 0; i < 19; ++i) kf[i] = (f32x2){bf_lo(kr[i][cp]), bf_hi(kr[i][cp])};
#pragma unroll
            for (int i = 0; i < 16; ++i) {
                f32x2 a = kf[i] * w3; a = __builtin_elementwise_fma(kf[i + 1], w2, a); a = __builtin_elementwise_fma(kf[i + 2], w1, a); a = __builtin_elementwise_fma(kf[i + 3], w0, a);
                o[i] = a * (f32x2){bf_lo(vr[i][cp]), bf_hi(vr[i][cp])};
            }
#pragma unroll
            for (int hh = 0; hh < 2; ++hh)
#pragma unroll
                for (int e = 0; e < 2; ++e) {
                    u32x4 pk; pk.x = cvt_pk_bf16(o[8 * hh + 7][e], o[8 * hh + 6][e]); pk.y = cvt_pk_bf16(o[8 * hh + 5][e], o[8 * hh + 4][e]);
                    pk.z = cvt_pk_bf16(o[8 * hh + 3][e], o[8 * hh + 2][e]); pk.w = cvt_pk_bf16(o[8 * hh + 1][e], o[8 * hh][e]);
                    const int chunk = 7 - (2 * rg + hh);
                    *(LAS u32x4*)(lds + (cg * 8 + 2 * cp + e) * 128 + ((chunk ^ (cg & 7)) * 16)) = pk;
                }
        }
    }
    STAMP();
    {
        const u32x4 g0 = *(const u32x4*)(gw + lane * 8), g1 = *(const u32x4*)(gw + 512 + lane * 8);
        u32x4 a0[8], a1[8];
#pragma unroll
        for (int r = 0; r < 8; ++r) { const u16* xr = x + (size_t)(64 * j + wid * 8 + r) * D_ + lane * 8; a0[r] = *(const u32x4*)xr; a1[r] = *(const u32x4*)(xr + 512); }
        float acc[8];
#pragma unroll
        for (int r = 0; r < 8; ++r) { float t = 0.f;
#pragma unroll
            for (int i = 0; i < 4; ++i) {
                t += rbf(bf_lo(a0[r][i]) * bf_lo(g0[i])); t += rbf(bf_hi(a0[r][i]) * bf_hi(g0[i]));
                t += rbf(bf_lo(a1[r][i]) * bf_lo(g1[i])); t += rbf(bf_hi(a1[r][i]) * bf_hi(g1[i]));
            }
            acc[r] = t; }
#pragma unroll
        for (int o = 32; o >= 1; o >>= 1)
#pragma unroll
            for (int r = 0; r < 8; ++r) acc[r] += __shfl_xor(acc[r], o);
        float z = acc[0];
#pragma unroll
        for (int r = 1; r < 8; ++r) z = (lane == r) ? acc[r] : z;
        if (lane < 8) ((float*)(ws + WS_G))[64 * j + wid * 8 + lane] = rbf(1.0f / (1.0f + __expf(-rbf(z))));
    }
    STAMP();
    __syncthreads();
    STAMP();
    const size_t xb = (size_t)(1984 - 64 * j) * 2;
#pragma unroll 4
    for (int it = 0; it < 16; ++it) {
        const int ch = it * 64 + (tid >> 3), c = tid & 7;
        const u32x4 t = *(const LAS u32x4*)(lds + ch * 128 + ((c ^ ((ch >> 3) & 7)) * 16));
        *(u32x4*)(ws + WS_UR + (size_t)ch * URP * 2 + xb + c * 16) = t;
    }
    STAMP();
    WAIT_V(0); STAMP(); STAMP_OUT(0);
}

__global__ __launch_bounds__(512, 2) void k1_gemm1_prep(const u16* __restrict__ x, const u16* __restrict__ kk, const u16* __restrict__ vv,
                                                        const u16* __restrict__ shk, const u16* __restrict__ wg, const u16* __restrict__ wu,
                                                        const u16* __restrict__ gw, const u16* __restrict__ ssmk, const u16* __restrict__ wd,
                                                        unsigned char* __restrict__ ws) {
    extern __shared__ __attribute__((aligned(16))) unsigned char smem[];
    LAS unsigned char* lds = (LAS unsigned char*)smem;
    const int xg = blockIdx.x & 7, l = blockIdx.x >> 3;
    if (l >= 28) { prep_block(lds, xg * 4 + (l - 28), x, kk, vv, shk, gw, wg, wu, ws); return; }
    STAMP_DECL; STAMP();
    const int pm = 4 * (xg & 1) + (l & 3), pn = 7 * (xg >> 1) + (l >> 2);
    const int tid = threadIdx.x, wid = __builtin_amdgcn_readfirstlane(tid >> 6), lane = tid & 63, wr = wid >> 2, wc = wid & 3, fr = lane & 15, fq = lane >> 4;
    constexpr int HTB = 16384;
    unsigned voffA[2], voffB[2];
#pragma unroll
    for (int i = 0; i < 2; ++i) {
        const int piece = wid + 8 * i;
        { const int row = piece * 8 + (lane >> 3), ch = (lane & 7) ^ (lane >> 3); voffA[i] = (unsigned)(row * 2048 + ch * 16); }
        { const int kr = piece * 4 + (lane >> 4), fs = (kr & 3) | (((kr >> 3) & 1) << 2), ch = (lane & 15) ^ (2 * fs); voffB[i] = (unsigned)(kr * (F_ * 2) + ch * 16); }
    }
    const unsigned ldsw = (unsigned)wid * 1024u;
    const int aoff0 = ((wr * 64 + fr) >> 3) * 1024 + (fr & 7) * 128 + ((fq ^ (fr & 7)) * 16), aoff1 = aoff0 ^ 64;
    const int tq = (lane & 15) >> 2, tp = lane & 3;
    const unsigned boff0 = (unsigned)(size_t)lds + 4 * HTB + (8 * fq + tq) * 256 + (((4 * wc + (tp >> 1)) ^ (2 * (tq | ((fq & 1) << 2)))) * 16) + 8 * (tp & 1), boff1 = boff0 ^ 32;
    const char* cA = (const char*)x + (size_t)pm * 256 * 2048;
    const char* cG = (const char*)wg + (size_t)pn * 256;
    const char* cU = (const char*)wu + (size_t)pn * 256;
    constexpr size_t kA = 128, kB = (size_t)64 * F_ * 2, hA = (size_t)128 * 2048;
#define SA(b, h) (((b) * 2 + (h)) * HTB)
#define SB(b, h) ((4 + (b) * 2 + (h)) * HTB)
#define STAGE(bufoff, gbase, voff) do { _Pragma("unroll") for (int _i = 0; _i < 2; ++_i) GLDS16((gbase) + (voff)[_i], (bufoff) + ldsw + _i * 8192); } while (0)
#define LDA(dst, b, h) do { _Pragma("unroll") for (int m = 0; m < 4; ++m) { dst[m][0] = LDS_B128(SA(b, h) + aoff0 + m * 2048); dst[m][1] = LDS_B128(SA(b, h) + aoff1 + m * 2048); } } while (0)
#define LDB(dst, b, h) do { \
        LDS_TR(dst[0], boff0, ((b) * 2 + (h)) * HTB); LDS_TR(dst[1], boff0, ((b) * 2 + (h)) * HTB + 1024); \
        LDS_TR(dst[2], boff0, ((b) * 2 + (h)) * HTB + 8192); LDS_TR(dst[3], boff0, ((b) * 2 + (h)) * HTB + 9216); \
        LDS_TR(dst[4], boff1, ((b) * 2 + (h)) * HTB); LDS_TR(dst[5], boff1, ((b) * 2 + (h)) * HTB + 1024); \
        LDS_TR(dst[6], boff1, ((b) * 2 + (h)) * HTB + 8192); LDS_TR(dst[7], boff1, ((b) * 2 + (h)) * HTB + 9216); } while (0)
#define WAIT_B(B) asm volatile("s_waitcnt lgkmcnt(0)" : "+v"(B[0]), "+v"(B[1]), "+v"(B[2]), "+v"(B[3]), "+v"(B[4]), "+v"(B[5]), "+v"(B[6]), "+v"(B[7]) :: "memory")
#define MMA(ai, bj, At, Bt) do { __builtin_amdgcn_s_setprio(1); _Pragma("unroll") for (int m = 0; m < 4; ++m) _Pragma("unroll") for (int n = 0; n < 2; ++n) _Pragma("unroll") for (int k = 0; k < 2; ++k) \
        acc[ai][bj][m][n] = __builtin_amdgcn_mfma_f32_16x16x32_bf16(CAT8(Bt[4 * n + 2 * k], Bt[4 * n + 2 * k + 1]), At[m][k], acc[ai][bj][m][n], 0, 0, 0); __builtin_amdgcn_s_setprio(0); } while (0)
    f32x4 acc[2][2][4][2];
#pragma unroll
    for (int a = 0; a < 2; ++a)
#pragma unroll
        for (int b = 0; b < 2; ++b)
#pragma unroll
            for (int m = 0; m < 4; ++m)
#pragma unroll
                for (int n = 0; n < 2; ++n) acc[a][b][m][n] = (f32x4){0.f, 0.f, 0.f, 0.f};
    bf16x8 At[4][2]; bf16x4 B0[8], B1[8];
    constexpr int nt = D_ / 64;
    STAGE(SB(0, 0), cG, voffB); STAGE(SA(0, 0), cA, voffA); STAGE(SB(0, 1), cU, voffB); STAGE(SA(0, 1), cA + hA, voffA);
    if (wr == 1) BAR();
    WAIT_V(4); BAR();
    STAGE(SB(1, 0), cG + kB, voffB); STAGE(SA(1, 0), cA + kA, voffA); STAGE(SB(1, 1), cU + kB, voffB);
    WAIT_V(6); BAR();
    STAMP();
    for (int t = 0; t < nt; t += 2) {
        const bool more = (t + 2 < nt);
        const char* a1 = cA + (size_t)(t + 1) * kA;
        const char* a2 = a1 + kA; const char* g2 = cG + (size_t)(t + 2) * kB; const char* u2 = cU + (size_t)(t + 2) * kB;
        const char* a3 = a2 + kA; const char* g3 = g2 + kB; const char* u3 = u2 + kB;
        LDB(B0, 0, 0); SCHED(); LDA(At, 0, 0); STAGE(SA(1, 1), a1 + hA, voffA);
        WAIT_L(8); BAR(); WAIT_B(B0); MMA(0, 0, At, B0); BAR(); SCHED();
        LDB(B1, 0, 1); if (more) STAGE(SB(0, 0), g2, voffB);
        BAR(); WAIT_B(B1); MMA(0, 1, At, B1); BAR();
        LDA(At, 0, 1); if (more) STAGE(SA(0, 0), a2, voffA);
        BAR(); WAIT_L(0); MMA(1, 0, At, B0); BAR(); SCHED();
        if (more) { STAGE(SB(0, 1), u2, voffB); WAIT_V(6); } else WAIT_V(0);
        BAR(); MMA(1, 1, At, B1); BAR();
        LDB(B0, 1, 0); SCHED(); LDA(At, 1, 0); if (more) STAGE(SA(0, 1), a2 + hA, voffA);
        WAIT_L(8); BAR(); WAIT_B(B0); MMA(0, 0, At, B0); BAR(); SCHED();
        LDB(B1, 1, 1); if (more) STAGE(SB(1, 0), g3, voffB);
        BAR(); WAIT_B(B1); MMA(0, 1, At, B1); BAR();
        LDA(At, 1, 1); if (more) STAGE(SA(1, 0), a3, voffA);
        BAR(); WAIT_L(0); MMA(1, 0, At, B0); BAR(); SCHED();
        if (more) { STAGE(SB(1, 1), u3, voffB); WAIT_V(6); }
        BAR(); MMA(1, 1, At, B1); BAR();
    }
    STAMP();
    {
        const int wgi = pm * 28 + pn;
        const char* pf = (const char*)wd + (size_t)wgi * 32768 + ldsw + lane * 16;
        const char* pf2 = (const char*)ssmk + (size_t)(wgi < 204 ? wgi : 204) * 20480 + (wid < 4 ? ldsw : 0u) + lane * 16;
        unsigned char* hp = ws + WS_H + ((size_t)(pm * 16 + wr * 4) * 112 + pn * 4 + wc) * 1024 + lane * 16;
#pragma unroll
        for (int ai = 0; ai < 2; ++ai)
#pragma unroll
            for (int m = 0; m < 4; ++m) {
                const f32x4 g0 = acc[ai][0][m][0], g1 = acc[ai][0][m][1], u0 = acc[ai][1][m][0], u1 = acc[ai][1][m][1];
                u32x4 o;
                o.x = cvt_pk_bf16(gelu_mul(g0[0], u0[0]), gelu_mul(g0[1], u0[1])); o.y = cvt_pk_bf16(gelu_mul(g0[2], u0[2]), gelu_mul(g0[3], u0[3]));
                o.z = cvt_pk_bf16(gelu_mul(g1[0], u1[0]), gelu_mul(g1[1], u1[1])); o.w = cvt_pk_bf16(gelu_mul(g1[2], u1[2]), gelu_mul(g1[3], u1[3]));
                *(u32x4*)(hp + (size_t)(ai * 8 + m) * 112 * 1024) = o;
                if (ai == 0) GLDS16(pf + m * 8192, 8 * HTB + ldsw);
                else if (m < 2) GLDS16(pf2 + ldsw * (wid >= 4) + m * 8192, 8 * HTB + ldsw);
                else if (m == 2 && wgi < 204) GLDS16(pf2 + 16384, 8 * HTB + ldsw);
            }
    }
    STAMP();
    WAIT_V(0);
    STAMP(); STAMP_OUT(0);
    if (wr == 0) BAR();
#undef SA
#undef SB
#undef STAGE
#undef LDA
#undef LDB
#undef MMA
#undef WAIT_B
}

constexpr int CV_UP = 4160, CV_HP = 4640, CV_HB = 4 * CV_UP, CV_CH = CV_HB + 2 * CV_HP;
constexpr int RB = 106496;
constexpr int NT2 = 28;
constexpr int CVF = 4;
constexpr int CV0 = CVF + 2;
constexpr int CVX = CV0 + 12, CVM = CVX + 1, CVS = CVM + 4, CVP = CVS + 2;
constexpr unsigned kSpinCap = 1u << 20;
constexpr int vm_b(int t) { return t + 2 < NT2 ? 2 : 0; }
constexpr int vm_a(int t) { return t + 3 < NT2 ? 2 : 0; }
constexpr int vm_f(int t) { return (t >= 0 && t < CVF) ? 3 : 0; }
constexpr int vm_s(int t) { return t == CVS ? 2 : 0; }
constexpr int vm_all(int t) { return vm_b(t) + vm_a(t) + vm_f(t) + vm_s(t); }
constexpr int vm_wait(int t) { return (t == CVF || t == CVS + 1) ? vm_all(t) : t + 2 < NT2 ? vm_a(t - 1) + vm_f(t - 1) + vm_s(t - 1) + vm_all(t) : 0; }
template <int N> __device__ __forceinline__ void wait_vm() { asm volatile("s_waitcnt vmcnt(%0)" :: "n"(N) : "memory"); }
__device__ constexpr int kSlA[3][12] = {{0,0,0,0,1,1,1,1,2,2,2,2}, {0,0,0,0,1,1,1,2,2,3,3,3}, {3,3,4,4,4,4,5,5,5,6,6,7}};
__device__ constexpr int kSlB[3][12] = {{0,1,2,3,0,1,2,3,0,1,2,3}, {4,5,6,7,4,5,6,4,5,4,3,2}, {0,1,0,1,2,3,0,1,2,0,1,0}};
constexpr int kUlo[3] = {0, 0, 3}, kUhi[3] = {2, 3, 7}, kHlo[3] = {0, 2, 0}, kHhi[3] = {3, 7, 3};

struct K2State {
    f32x4 acc[2][8];
    bf16x8 A[4][2];
    bf16x4 B[16];
    f32x4 cacc[8];
};

__global__ __launch_bounds__(512, 2) void k2_conv_gemm2(const u16* __restrict__ q, const u16* __restrict__ ssmk, const u16* __restrict__ ssmD,
                                                        const u16* __restrict__ wd, unsigned char* __restrict__ ws, u16* __restrict__ out) {
    extern __shared__ __attribute__((aligned(16))) unsigned char smem[];
    LAS unsigned char* lds = (LAS unsigned char*)smem;
    const unsigned lbase = (unsigned)(size_t)lds;
    const int tid = threadIdx.x, wid = __builtin_amdgcn_readfirstlane(tid >> 6), lane = tid & 63, fr = lane & 15, fq = lane >> 4;
    const int bid = blockIdx.x, xg = bid & 7, l = bid >> 3;
    const int z = xg & 1, tm = 8 * ((xg >> 1) & 1) + (l & 7), tn = 4 * (xg >> 2) + (l >> 3);
    const int kh = wid >> 2, wm = wid & 3;
    STAMP_DECL; STAMP();
    unsigned voffB[2];
#pragma unroll
    for (int i = 0; i < 2; ++i) { const int kr = 32 * kh + (wm + 4 * i) * 4 + (lane >> 4), ch = (lane & 15) ^ (2 * (kr & 7)); voffB[i] = (unsigned)(kr * 2048 + ch * 16); }
    const unsigned ldsw = (unsigned)(kh * 8192 + wm * 1024), voffA = (unsigned)lane * 16u;
    const char* cA0 = (const char*)(ws + WS_H) + ((size_t)(tm * 8 + wm * 2) * 112 + z * 56 + kh) * 1024;
    const char* cA1 = cA0 + 112 * 1024;
    const char* cB = (const char*)wd + (size_t)z * 1792 * 2048 + (size_t)tn * 256;
    constexpr size_t kB = (size_t)64 * 2048;
    K2State st; SEG_DECL;
#define STAGEB(t) do { const char* _b = cB + (size_t)(t) * kB; GLDS16(_b + voffB[0], RB + ((t) % 3) * 16384 + ldsw); GLDS16(_b + voffB[1], RB + ((t) % 3) * 16384 + ldsw + 4096); } while (0)
#define LOADA(t) do { asm volatile("global_load_dwordx4 %0, %1, %2" : "=v"(st.A[(t) & 3][0]) : "v"(voffA), "s"(cA0 + (size_t)(t) * 2048) : "memory"); \
        asm volatile("global_load_dwordx4 %0, %1, %2" : "=v"(st.A[(t) & 3][1]) : "v"(voffA), "s"(cA1 + (size_t)(t) * 2048) : "memory"); } while (0)
    const int ch = wid & 3, hf = wid >> 2, d = bid * 4 + ch, CB = ch * CV_CH;
    const unsigned dpair = ((const unsigned*)ssmD)[d >> 1];
    const float dval = (d & 1) ? bf_hi(dpair) : bf_lo(dpair);
    const u32x2 h03 = *(const u32x2*)(ssmk + (size_t)d * S_);
    STAGEB(0); LOADA(0); STAGEB(1); LOADA(1); LOADA(2);
    const char* ur = (const char*)(ws + WS_UR) + (size_t)d * URP * 2 + (3 - 2 * hf) * 2 + lane * 16;
    const auto hrs = __builtin_amdgcn_make_buffer_rsrc((void*)ssmk, 0, D_ * S_ * 2, 0x00020000);
    const int hvo = d * (S_ * 2) + (1 - hf) * 8 + lane * 16;
#define CFILL(i) do { if constexpr ((i) < 8) GLDS16(ur - ((i) >> 2) * 2 + ((i) & 3) * 1024, CB + (2 * hf + ((i) >> 2)) * CV_UP + ((i) & 3) * 1024); \
        else __builtin_amdgcn_raw_ptr_buffer_load_lds(hrs, (LAS void*)(lds + CB + CV_HB + hf * CV_HP + (29 - hf) * 16 + ((i) & 3) * 1024), 16, hvo + ((i) & 3) * 1024, 0, 0, 0); } while (0)
    if (lane < 28) *(LAS u32x4*)(lds + CB + CV_HB + hf * CV_HP + lane * 16) = (u32x4){0u, 0u, 0u, 0u};
    else if (lane < 34) { const int e = lane - 28; *(LAS u32x4*)(lds + CB + (2 * hf + e / 3) * CV_UP + 4096 + (e % 3) * 16) = (u32x4){0u, 0u, 0u, 0u}; }
    else if (lane == 34 && hf == 0) *(LAS u32x4*)(lds + CB + CV_HB + 28 * 16) = (u32x4){0u, 0u, h03.x, h03.y};
    const int tq = fr >> 2, tp4 = lane & 3;
    unsigned boff[8];
    { const unsigned b0 = lbase + RB + (32 * kh + 4 * fq + tq) * 256 + 8 * (tp4 & 1) + (((tp4 >> 1) ^ (2 * ((4 * fq + tq) & 7))) * 16);
#pragma unroll
      for (int n = 0; n < 8; ++n) boff[n] = b0 ^ (32 * n); }
    const unsigned ua = lbase + CB + (fr & 3) * CV_UP + 2 * (2040 - 8 * (fr >> 2) + 8 * fq) - 64 * 59 - 256 * hf;
    const unsigned ha = lbase + CB + CV_HB + (fr & 1) * CV_HP + 2 * (224 + 32 * (fr >> 1) + 8 * fq) - 192 - 256 * hf;
#pragma unroll
    for (int m = 0; m < 2; ++m)
#pragma unroll
        for (int n = 0; n < 8; ++n) st.acc[m][n] = (f32x4){0.f, 0.f, 0.f, 0.f};
#pragma unroll
    for (int n = 0; n < 8; ++n) st.cacc[n] = (f32x4){0.f, 0.f, 0.f, 0.f};
#define RDB(t, n) do { LDS_TR(st.B[2 * (n)], boff[n], ((t) % 3) * 16384); LDS_TR(st.B[2 * (n) + 1], boff[n], ((t) % 3) * 16384 + 4096); } while (0)
#define RD128(dst, vaddr, imm) asm volatile("ds_read_b128 %0, %1 offset:%2" : "=v"(dst) : "v"(vaddr), "i"(imm) : "memory")
    gu32* flags = (gu32*)(ws + WS_FL);
    const auto yrs = __builtin_amdgcn_make_buffer_rsrc((void*)(ws + WS_Y2 + (size_t)bid * 16384), 0, 16384, 0x00020000);
    wait_vm<6>(); WAIT_L(0); BAR();
    if (kh) BAR();
    STAMP();

    auto trip = [&](auto tc) __attribute__((always_inline)) {
        constexpr int t = decltype(tc)::value;
        if constexpr (t == CV0 || t == CV0 + 6 || t == CVX || t == CVS + 1) STAMP();
        constexpr bool cv = (t >= CV0 && t < CVX);
        constexpr int cc = cv ? (t - CV0) / 3 : 0, sl = cv ? (t - CV0) % 3 : 0;
        constexpr int sgrp = cv ? 1 : (t >= CVX && t <= CVS) ? 2 : 0;
        SEG(sgrp, 0);
        bf16x8 U[8], Hf[8];
        const char* bsrc = cB + (size_t)(t + 2) * kB;
        RDB(t, 0); RDB(t, 1);
        if constexpr (vm_b(t) != 0) GLDS16(bsrc + voffB[0], RB + ((t + 2) % 3) * 16384 + ldsw);
        SCHED(); RDB(t, 2); RDB(t, 3);
        if constexpr (vm_b(t) != 0) GLDS16(bsrc + voffB[1], RB + ((t + 2) % 3) * 16384 + ldsw + 4096);
        SCHED(); RDB(t, 4); RDB(t, 5);
        if constexpr (vm_a(t) != 0) asm volatile("global_load_dwordx4 %0, %1, %2" : "=v"(st.A[(t + 3) & 3][0]) : "v"(voffA), "s"(cA0 + (size_t)(t + 3) * 2048) : "memory");
        RDB(t, 6); RDB(t, 7);
        if constexpr (vm_a(t) != 0) asm volatile("global_load_dwordx4 %0, %1, %2" : "=v"(st.A[(t + 3) & 3][1]) : "v"(voffA), "s"(cA1 + (size_t)(t + 3) * 2048) : "memory");
        if constexpr (cv) {
#pragma unroll
            for (int a = kUlo[sl]; a <= kUhi[sl]; ++a) RD128(U[a], ua, 64 * (59 - 8 * a - cc));
#pragma unroll
            for (int b = kHlo[sl]; b <= kHhi[sl]; ++b) RD128(Hf[b], ha, 64 * (8 * b - cc + 3));
        }
        if constexpr (vm_f(t) != 0) { CFILL(3 * t); CFILL(3 * t + 1); CFILL(3 * t + 2); }
        if constexpr (t == CVX) {
#pragma unroll
            for (int i = 0; i < 4; ++i) *(LAS f32x4*)(lds + CB + CV_HB + hf * CV_HP + lane * 16 + i * 1024) = hf ? st.cacc[i] : st.cacc[4 + i];
        }
        if constexpr (t >= CVM && t < CVM + 4) {
            {
                constexpr int i = t - CVM;
                const f32x4 y = (hf ? st.cacc[4 + i] : st.cacc[i]) + *(const LAS f32x4*)(lds + CB + CV_HB + (1 - hf) * CV_HP + lane * 16 + i * 1024);
                const int s0 = 256 * (4 * hf + i) + 32 * (fr >> 1) + 4 * (fr & 1) + 8 * fq;
                const u32x2 kv = *(const LAS u32x2*)(lds + CB + 3 * CV_UP + 2 * (2044 - s0));
                const float k3 = bf_lo(kv.x), k2 = bf_hi(kv.x), k1 = bf_lo(kv.y), k0 = bf_hi(kv.y);
                const unsigned p01 = cvt_pk_bf16(rbf(y[0]) + rbf(k0 * dval), rbf(y[1]) + rbf(k1 * dval));
                const unsigned p23 = cvt_pk_bf16(rbf(y[2]) + rbf(k2 * dval), rbf(y[3]) + rbf(k3 * dval));
                LAS u16* tp = (LAS u16*)(lds + (2 * hf + (i >> 1)) * CV_CH + (s0 & 511) * 8 + ((s0 & 511) >> 5) * 8 + ch * 2);
                tp[0] = (u16)p01; tp[4] = (u16)(p01 >> 16); tp[8] = (u16)p23; tp[12] = (u16)(p23 >> 16);
            }
        }
        if constexpr (t == CVS) {
#pragma unroll
            for (int i = 0; i < 2; ++i) { const int s = (i * 512 + tid) * 2; const int o = (s >> 9) * CV_CH + (s & 511) * 8 + ((s & 511) >> 5) * 8;
                const u32x2 y0 = *(const LAS u32x2*)(lds + o), y1 = *(const LAS u32x2*)(lds + o + 8);
                __builtin_amdgcn_raw_buffer_store_b128((u32x4){y0.x, y0.y, y1.x, y1.y}, yrs, (i * 512 + tid) * 16, 0, 16);
            }
        }
        if constexpr (t == CVP) { if (tid == 256) __hip_atomic_store(flags + bid * 16, 1u, RLX_AGENT); }
        asm volatile("s_waitcnt lgkmcnt(0)" : "+v"(st.B[0]), "+v"(st.B[1]), "+v"(st.B[2]), "+v"(st.B[3]), "+v"(st.B[4]), "+v"(st.B[5]), "+v"(st.B[6]), "+v"(st.B[7]),
                     "+v"(st.B[8]), "+v"(st.B[9]), "+v"(st.B[10]), "+v"(st.B[11]), "+v"(st.B[12]), "+v"(st.B[13]), "+v"(st.B[14]), "+v"(st.B[15]),
                     "+v"(st.A[t & 3][0]), "+v"(st.A[t & 3][1]) :: "memory");
        if constexpr (cv && sl == 0) asm volatile("" : "+v"(U[0]), "+v"(U[1]), "+v"(U[2]), "+v"(Hf[0]), "+v"(Hf[1]), "+v"(Hf[2]), "+v"(Hf[3]) :: "memory");
        if constexpr (cv && sl == 1) asm volatile("" : "+v"(U[0]), "+v"(U[1]), "+v"(U[2]), "+v"(U[3]), "+v"(Hf[2]), "+v"(Hf[3]), "+v"(Hf[4]), "+v"(Hf[5]), "+v"(Hf[6]), "+v"(Hf[7]) :: "memory");
        if constexpr (cv && sl == 2) asm volatile("" : "+v"(U[3]), "+v"(U[4]), "+v"(U[5]), "+v"(U[6]), "+v"(U[7]), "+v"(Hf[0]), "+v"(Hf[1]), "+v"(Hf[2]), "+v"(Hf[3]) :: "memory");
        SEG(sgrp, 1);
        SCHED(); BAR(); SCHED();
        SEG(sgrp, 2);
        __builtin_amdgcn_s_setprio(1);
#pragma unroll
        for (int n = 0; n < 8; ++n)
#pragma unroll
            for (int m = 0; m < 2; ++m)
                st.acc[m][n] = __builtin_amdgcn_mfma_f32_16x16x32_bf16(CAT8(st.B[2 * n], st.B[2 * n + 1]), st.A[t & 3][m], st.acc[m][n], 0, 0, 0);
        if constexpr (cv) {
#pragma unroll
            for (int i = 0; i < 12; ++i)
                st.cacc[kSlA[sl][i] + kSlB[sl][i]] = __builtin_amdgcn_mfma_f32_16x16x32_bf16(U[kSlA[sl][i]], Hf[kSlB[sl][i]], st.cacc[kSlA[sl][i] + kSlB[sl][i]], 0, 0, 0);
        }
        __builtin_amdgcn_s_setprio(0);
        SCHED();
        SEG(sgrp, 3);
        if constexpr (t + 1 < NT2) { wait_vm<vm_wait(t)>(); SEG(sgrp, 4); BAR(); SEG(sgrp, 5); } else { if (!kh) BAR(); }
        SCHED();
    };
#define TRIP4(t) trip(std::integral_constant<int, (t)>{}); trip(std::integral_constant<int, (t) + 1>{}); trip(std::integral_constant<int, (t) + 2>{}); trip(std::integral_constant<int, (t) + 3>{})
    TRIP4(0); TRIP4(4); TRIP4(8); TRIP4(12); TRIP4(16); TRIP4(20); TRIP4(24);
    STAMP();
    {
        const int s = tm * 128 + wm * 32 + z * 16 + fr, dcol = tn * 128 + kh * 64 + 4 * fq;
        u32x2 qv[4];
#pragma unroll
        for (int n = 0; n < 4; ++n) qv[n] = *(const u32x2*)(q + (size_t)s * D_ + dcol + 16 * n);
        const float g = ((const float*)(ws + WS_G))[s];
        gu32* fp = flags + ((lane < 16) ? (tn * 32 + kh * 16 + lane) : (256 + (bid ^ 1) * 8 + wid)) * 16;
        unsigned fv = __hip_atomic_load(fp, RLX_AGENT);
        const int xw = wid * 8192 + lane * 16, xr = (wid ^ 4) * 8192 + lane * 16;
#pragma unroll
        for (int n = 0; n < 4; ++n) {
            *(LAS f32x4*)(lds + xw + n * 1024) = z ? (kh ? st.acc[0][n] : st.acc[0][4 + n]) : (kh ? st.acc[1][n] : st.acc[1][4 + n]);
            *(LAS f32x4*)(lds + xw + (4 + n) * 1024) = z ? (kh ? st.acc[1][n] : st.acc[1][4 + n]) : (kh ? st.acc[0][n] : st.acc[0][4 + n]);
        }
        WAIT_L(0); BAR();
        const auto xs = __builtin_amdgcn_make_buffer_rsrc((void*)(ws + WS_X + (size_t)bid * 32768), 0, 32768, 0x00020000);
#pragma unroll
        for (int n = 0; n < 4; ++n) {
            const f32x4 own = z ? (kh ? st.acc[0][4 + n] : st.acc[0][n]) : (kh ? st.acc[1][4 + n] : st.acc[1][n]);
            __builtin_amdgcn_raw_buffer_store_b128(__builtin_bit_cast(u32x4, own + *(const LAS f32x4*)(lds + xr + n * 1024)), xs,
                                                   wid * 4096 + n * 1024 + lane * 16, 0, 16);
        }
        f32x4 keep[4];
#pragma unroll
        for (int n = 0; n < 4; ++n)
            keep[n] = (z ? (kh ? st.acc[1][4 + n] : st.acc[1][n]) : (kh ? st.acc[0][4 + n] : st.acc[0][n])) + *(const LAS f32x4*)(lds + xr + (4 + n) * 1024);
        STAMP();
        const auto xp = __builtin_amdgcn_make_buffer_rsrc((void*)(ws + WS_X + (size_t)(bid ^ 1) * 32768), 0, 32768, 0x00020000);
        const auto y2 = __builtin_amdgcn_make_buffer_rsrc((void*)(ws + WS_Y2), 0, 256 * S_ * 8, 0x00020000);
        unsigned spins = 0;
#pragma nounroll
        while (!__all(lane >= 16 || fv == 1u) && ++spins < kSpinCap) { asm volatile("s_sleep 1" ::: "memory"); fv = __hip_atomic_load(fp, RLX_AGENT); }
        asm volatile("" ::: "memory");
        u32x2 yv[4];
#pragma unroll
        for (int n = 0; n < 4; ++n) yv[n] = __builtin_amdgcn_raw_buffer_load_b64(y2, (((dcol >> 2) + 4 * n) * S_ + s) * 8, 0, 16);
        WAIT_V(0);
        if (lane == 0) __hip_atomic_store(flags + (256 + bid * 8 + wid) * 16, 1u, RLX_AGENT);
        STAMP();
#pragma nounroll
        do { fv = __hip_atomic_load(fp, RLX_AGENT); } while (!__all(lane != 16 || fv == 1u) && ++spins < kSpinCap);
        asm volatile("" ::: "memory");
        u32x4 pr[4];
#pragma unroll
        for (int n = 0; n < 4; ++n) pr[n] = __builtin_amdgcn_raw_buffer_load_b128(xp, wid * 4096 + n * 1024 + lane * 16, 0, 16);
        STAMP();
        const float omg = rbf(1.0f - g);
#pragma unroll
        for (int n = 0; n < 4; ++n) {
            const f32x4 b = keep[n] + __builtin_bit_cast(f32x4, pr[n]);
            const float a0 = rbf(bf_lo(qv[n].x) * bf_lo(yv[n].x)), a1 = rbf(bf_hi(qv[n].x) * bf_hi(yv[n].x));
            const float a2 = rbf(bf_lo(qv[n].y) * bf_lo(yv[n].y)), a3 = rbf(bf_hi(qv[n].y) * bf_hi(yv[n].y));
            u32x2 o;
            o.x = cvt_pk_bf16(rbf(g * a0) + rbf(omg * rbf(b[0])), rbf(g * a1) + rbf(omg * rbf(b[1])));
            o.y = cvt_pk_bf16(rbf(g * a2) + rbf(omg * rbf(b[2])), rbf(g * a3) + rbf(omg * rbf(b[3])));
            *(u32x2*)(out + (size_t)s * D_ + dcol + 16 * n) = o;
        }
        STAMP(); WAIT_V(0); STAMP(); STAMP_OUT(1); SEG_OUT();
    }
#undef STAGEB
#undef LOADA
#undef RDB
#undef CFILL
#undef RD128
#undef TRIP4
}

extern "C" void kernel_launch(void* const* d_in, const int* in_sizes, int n_in, void* d_out, int out_size, void* d_ws, size_t ws_size,
                              hipStream_t stream) {
    const u16* q = (const u16*)d_in[0];
    const u16* k = (const u16*)d_in[1];
    const u16* v = (const u16*)d_in[2];
    const u16* shk = (const u16*)d_in[3];
    const u16* ssmk = (const u16*)d_in[4];
    const u16* ssmD = (const u16*)d_in[5];
    const u16* wg = (const u16*)d_in[7];
    const u16* wu = (const u16*)d_in[8];
    const u16* wd = (const u16*)d_in[9];
    const u16* gw = (const u16*)d_in[10];
    (void)hipFuncSetAttribute((const void*)k1_gemm1_prep, hipFuncAttributeMaxDynamicSharedMemorySize, K1_LDS);
    (void)hipFuncSetAttribute((const void*)k2_conv_gemm2, hipFuncAttributeMaxDynamicSharedMemorySize, K2_LDS);
    k1_gemm1_prep<<<256, 512, K1_LDS, stream>>>(q, k, v, shk, wg, wu, gw, ssmk, wd, (unsigned char*)d_ws);
    k2_conv_gemm2<<<256, 512, K2_LDS, stream>>>(q, ssmk, ssmD, wd, (unsigned char*)d_ws, (u16*)d_out);
}
```

```cpp
#include <hip/hip_runtime.h>
#include <hip/hip_bf16.h>
#include <type_traits>

#define LAS __attribute__((address_space(3)))
#define GAS __attribute__((address_space(1)))
typedef unsigned short u16;
typedef short bf16x8 __attribute__((ext_vector_type(8)));
typedef short bf16x4 __attribute__((ext_vector_type(4)));
typedef float f32x4 __attribute__((ext_vector_type(4)));
typedef float f32x2 __attribute__((ext_vector_type(2)));
typedef unsigned u32x4 __attribute__((ext_vector_type(4)));
typedef unsigned u32x2 __attribute__((ext_vector_type(2)));
typedef GAS unsigned gu32;
typedef GAS unsigned long long gu64;
#define RLX_AGENT __ATOMIC_RELAXED, __HIP_MEMORY_SCOPE_AGENT

constexpr int S_ = 2048, D_ = 1024, F_ = 3584;
constexpr int URP = 2112;
constexpr size_t WS_H = 0;
constexpr size_t WS_UR = WS_H + (size_t)S_ * F_ * 2;
constexpr size_t WS_G = WS_UR + (size_t)D_ * URP * 2;
constexpr size_t WS_Y2 = WS_G + (size_t)S_ * 4;
constexpr size_t WS_X = WS_Y2 + (size_t)256 * S_ * 8;
constexpr size_t WS_FL = WS_X + (size_t)256 * 65536;
constexpr int K1_LDS = 139264, K2_LDS = 163840;

__device__ __forceinline__ unsigned cvt_pk_bf16(float lo, float hi) {
    unsigned r; asm("v_cvt_pk_bf16_f32 %0, %1, %2" : "=v"(r) : "v"(lo), "v"(hi)); return r;
}
__device__ __forceinline__ float bf_lo(unsigned w) { return __uint_as_float(w << 16); }
__device__ __forceinline__ float bf_hi(unsigned w) { return __uint_as_float(w & 0xffff0000u); }
__device__ __forceinline__ float rbf(float x) { return bf_lo(cvt_pk_bf16(x, x)); }

#define GLDS16(gptr, ldsoff) __builtin_amdgcn_global_load_lds((const unsigned*)(gptr), (LAS unsigned*)(lds + (ldsoff)), 16, 0, 0)
#define LDS_B128(off) (*(const LAS bf16x8*)(lds + (off)))
#define LDS_TR(dst, vaddr, imm) asm volatile("ds_read_b64_tr_b16 %0, %1 offset:%2" : "=v"(dst) : "v"(vaddr), "i"(imm) : "memory")
#define CAT8(a, b) __builtin_shufflevector(a, b, 0, 1, 2, 3, 4, 5, 6, 7)
#define WAIT_V(n) asm volatile("s_waitcnt vmcnt(" #n ")" ::: "memory")
#define WAIT_L(n) asm volatile("s_waitcnt lgkmcnt(" #n ")" ::: "memory")
#define BAR() do { asm volatile("" ::: "memory"); __builtin_amdgcn_s_barrier(); asm volatile("" ::: "memory"); } while (0)
#define SCHED() __builtin_amdgcn_sched_barrier(0)

#define STAMP_DECL
#define STAMP()
#define STAMP_OUT(kidx)
#define SEG_DECL
#define SEG(grp, i)
#define SEG_OUT()
__device__ __forceinline__ unsigned gelu_mul2(f32x2 g, f32x2 u) {
    const f32x2 p = __builtin_elementwise_fma(g * g, (f32x2){-0.044715f * 2.3022082f, -0.044715f * 2.3022082f}, (f32x2){-2.3022082f, -2.3022082f});
    const f32x2 y = g * p;
    const f32x2 dd = (f32x2){__builtin_amdgcn_exp2f(fminf(y.x, 60.f)), __builtin_amdgcn_exp2f(fminf(y.y, 60.f))} + 1.0f;
    const float r = __builtin_amdgcn_rcpf(dd.x * dd.y);
    const f32x2 h = (g * u) * ((f32x2){dd.y, dd.x} * r);
    return cvt_pk_bf16(h.x, h.y);
}

__device__ __forceinline__ void prep_block(LAS unsigned char* lds, int j, const u16* x, const u16* kk, const u16* vv, const u16* shk, const u16* gw,
                                           const u16* wg, const u16* wu, unsigned char* ws) {
    const int tid = threadIdx.x, wid = __builtin_amdgcn_readfirstlane(tid >> 6), lane = tid & 63;
    const unsigned ldsw = (unsigned)wid * 1024u;
    STAMP_DECL; STAMP();
    if (tid < 72) *(unsigned*)(ws + WS_FL + (size_t)(j * 72 + tid) * 64) = 0u;
    if (tid >= 256) { const int t = tid - 256; *(u32x4*)(ws + WS_UR + ((size_t)(j * 32 + (t >> 3)) * URP + 2048) * 2 + (t & 7) * 16) = (u32x4){0u, 0u, 0u, 0u}; }
    {
        const char* w = (const char*)((wid < 4) ? wg : wu) + (size_t)j * 14336 + (wid & 3) * 1024 + lane * 16;
        const char* xs = (const char*)x + (size_t)(64 * j + 8 * wid + (lane >> 3)) * 2048 + (lane & 7) * 16;
#pragma unroll 1
        for (int t = 2; t < 16; ++t) {
            const char* p = w + (size_t)t * 64 * 7168;
            GLDS16(xs + t * 128, ldsw); GLDS16(p, ldsw); GLDS16(p + 4096, ldsw); GLDS16(p + 8192, ldsw);
            if ((wid & 3) < 2) GLDS16(p + 12288, ldsw);
            WAIT_V(24);
        }
    }
    WAIT_V(0);
    __syncthreads();
    STAMP();
    const int cg = tid & 127, rg = tid >> 7;
    {
        const int s0 = 64 * j + 16 * rg;
        u32x4 kr[19], vr[16], wr4[4];
#pragma unroll
        for (int i = 0; i < 4; ++i) wr4[i] = ((const u32x4*)(shk + cg * 32))[i];
#pragma unroll
        for (int i = 0; i < 19; ++i) { const int s = s0 - 3 + i; kr[i] = *(const u32x4*)(kk + (size_t)(s < 0 ? 0 : s) * D_ + cg * 8); }
#pragma unroll
        for (int i = 0; i < 16; ++i) vr[i] = *(const u32x4*)(vv + (size_t)(s0 + i) * D_ + cg * 8);
        if (s0 == 0) kr[0] = kr[1] = kr[2] = (u32x4){0u, 0u, 0u, 0u};
#pragma unroll
        for (int cp = 0; cp < 4; ++cp) {
            const f32x2 w0 = {bf_lo(wr4[cp].x), bf_lo(wr4[cp].z)}, w1 = {bf_hi(wr4[cp].x), bf_hi(wr4[cp].z)};
            const f32x2 w2 = {bf_lo(wr4[cp].y), bf_lo(wr4[cp].w)}, w3 = {bf_hi(wr4[cp].y), bf_hi(wr4[cp].w)};
            f32x2 kf[19], o[16];
#pragma unroll
            for (int i = 0; i < 19; ++i) kf[i] = (f32x2){bf_lo(kr[i][cp]), bf_hi(kr[i][cp])};
#pragma unroll
            for (int i = 0; i < 16; ++i) {
                f32x2 a = kf[i] * w3; a = __builtin_elementwise_fma(kf[i + 1], w2, a); a = __builtin_elementwise_fma(kf[i + 2], w1, a); a = __builtin_elementwise_fma(kf[i + 3], w0, a);
                o[i] = a * (f32x2){bf_lo(vr[i][cp]), bf_hi(vr[i][cp])};
            }
#pragma unroll
            for (int hh = 0; hh < 2; ++hh)
#pragma unroll
                for (int e = 0; e < 2; ++e) {
                    u32x4 pk; pk.x = cvt_pk_bf16(o[8 * hh + 7][e], o[8 * hh + 6][e]); pk.y = cvt_pk_bf16(o[8 * hh + 5][e], o[8 * hh + 4][e]);
                    pk.z = cvt_pk_bf16(o[8 * hh + 3][e], o[8 * hh + 2][e]); pk.w = cvt_pk_bf16(o[8 * hh + 1][e], o[8 * hh][e]);
                    const int chunk = 7 - (2 * rg + hh);
                    *(LAS u32x4*)(lds + (cg * 8 + 2 * cp + e) * 128 + ((chunk ^ (cg & 7)) * 16)) = pk;
                }
        }
    }
    STAMP();
    {
        const u32x4 g0 = *(const u32x4*)(gw + lane * 8), g1 = *(const u32x4*)(gw + 512 + lane * 8);
        u32x4 a0[8], a1[8];
#pragma unroll
        for (int r = 0; r < 8; ++r) { const u16* xr = x + (size_t)(64 * j + wid * 8 + r) * D_ + lane * 8; a0[r] = *(const u32x4*)xr; a1[r] = *(const u32x4*)(xr + 512); }
        float acc[8];
#pragma unroll
        for (int r = 0; r < 8; ++r) { float t = 0.f;
#pragma unroll
            for (int i = 0; i < 4; ++i) {
                t += rbf(bf_lo(a0[r][i]) * bf_lo(g0[i])); t += rbf(bf_hi(a0[r][i]) * bf_hi(g0[i]));
                t += rbf(bf_lo(a1[r][i]) * bf_lo(g1[i])); t += rbf(bf_hi(a1[r][i]) * bf_hi(g1[i]));
            }
            acc[r] = t; }
#pragma unroll
        for (int o = 32; o >= 1; o >>= 1)
#pragma unroll
            for (int r = 0; r < 8; ++r) acc[r] += __shfl_xor(acc[r], o);
        float z = acc[0];
#pragma unroll
        for (int r = 1; r < 8; ++r) z = (lane == r) ? acc[r] : z;
        if (lane < 8) ((float*)(ws + WS_G))[64 * j + wid * 8 + lane] = rbf(1.0f / (1.0f + __expf(-rbf(z))));
    }
    STAMP();
    __syncthreads();
    STAMP();
    const size_t xb = (size_t)(1984 - 64 * j) * 2;
#pragma unroll 4
    for (int it = 0; it < 16; ++it) {
        const int ch = it * 64 + (tid >> 3), c = tid & 7;
        const u32x4 t = *(const LAS u32x4*)(lds + ch * 128 + ((c ^ ((ch >> 3) & 7)) * 16));
        *(u32x4*)(ws + WS_UR + (size_t)ch * URP * 2 + xb + c * 16) = t;
    }
    STAMP();
    WAIT_V(0); STAMP(); STAMP_OUT(0);
}

__global__ __launch_bounds__(512, 2) void k1_gemm1_prep(const u16* __restrict__ x, const u16* __restrict__ kk, const u16* __restrict__ vv,
                                                        const u16* __restrict__ shk, const u16* __restrict__ wg, const u16* __restrict__ wu,
                                                        const u16* __restrict__ gw, const u16* __restrict__ ssmk, const u16* __restrict__ wd,
                                                        unsigned char* __restrict__ ws) {
    extern __shared__ __attribute__((aligned(16))) unsigned char smem[];
    LAS unsigned char* lds = (LAS unsigned char*)smem;
    const int xg = blockIdx.x & 7, l = blockIdx.x >> 3;
    if (l >= 28) { prep_block(lds, xg * 4 + (l - 28), x, kk, vv, shk, gw, wg, wu, ws); return; }
    STAMP_DECL; STAMP();
    const int pm = 4 * (xg & 1) + (l & 3), pn = 7 * (xg >> 1) + (l >> 2);
    const int tid = threadIdx.x, wid = __builtin_amdgcn_readfirstlane(tid >> 6), lane = tid & 63, wr = wid >> 2, wc = wid & 3, fr = lane & 15, fq = lane >> 4;
    constexpr int HTB = 16384;
    unsigned voffA[2], voffB[2];
#pragma unroll
    for (int i = 0; i < 2; ++i) {
        const int piece = wid + 8 * i;
        { const int row = piece * 8 + (lane >> 3), ch = (lane & 7) ^ (lane >> 3); voffA[i] = (unsigned)(row * 2048 + ch * 16); }
        { const int kr = piece * 4 + (lane >> 4), fs = (kr & 3) | (((kr >> 3) & 1) << 2), ch = (lane & 15) ^ (2 * fs); voffB[i] = (unsigned)(kr * (F_ * 2) + ch * 16); }
    }
    const unsigned ldsw = (unsigned)wid * 1024u;
    const int aoff0 = ((wr * 64 + fr) >> 3) * 1024 + (fr & 7) * 128 + ((fq ^ (fr & 7)) * 16), aoff1 = aoff0 ^ 64;
    const int tq = (lane & 15) >> 2, tp = lane & 3;
    const unsigned boff0 = (unsigned)(size_t)lds + 4 * HTB + (8 * fq + tq) * 256 + (((4 * wc + (tp >> 1)) ^ (2 * (tq | ((fq & 1) << 2)))) * 16) + 8 * (tp & 1), boff1 = boff0 ^ 32;
    const char* cA = (const char*)x + (size_t)pm * 256 * 2048;
    const char* cG = (const char*)wg + (size_t)pn * 256;
    const char* cU = (const char*)wu + (size_t)pn * 256;
    constexpr size_t kA = 128, kB = (size_t)64 * F_ * 2, hA = (size_t)128 * 2048;
#define SA(b, h) (((b) * 2 + (h)) * HTB)
#define SB(b, h) ((4 + (b) * 2 + (h)) * HTB)
#define STAGE(bufoff, gbase, voff) do { _Pragma("unroll") for (int _i = 0; _i < 2; ++_i) GLDS16((gbase) + (voff)[_i], (bufoff) + ldsw + _i * 8192); } while (0)
#define LDA(dst, b, h) do { _Pragma("unroll") for (int m = 0; m < 4; ++m) { dst[m][0] = LDS_B128(SA(b, h) + aoff0 + m * 2048); dst[m][1] = LDS_B128(SA(b, h) + aoff1 + m * 2048); } } while (0)
#define LDB(dst, b, h) do { \
        LDS_TR(dst[0], boff0, ((b) * 2 + (h)) * HTB); LDS_TR(dst[1], boff0, ((b) * 2 + (h)) * HTB + 1024); \
        LDS_TR(dst[2], boff0, ((b) * 2 + (h)) * HTB + 8192); LDS_TR(dst[3], boff0, ((b) * 2 + (h)) * HTB + 9216); \
        LDS_TR(dst[4], boff1, ((b) * 2 + (h)) * HTB); LDS_TR(dst[5], boff1, ((b) * 2 + (h)) * HTB + 1024); \
        LDS_TR(dst[6], boff1, ((b) * 2 + (h)) * HTB + 8192); LDS_TR(dst[7], boff1, ((b) * 2 + (h)) * HTB + 9216); } while (0)
#define WAIT_B(B) asm volatile("s_waitcnt lgkmcnt(0)" : "+v"(B[0]), "+v"(B[1]), "+v"(B[2]), "+v"(B[3]), "+v"(B[4]), "+v"(B[5]), "+v"(B[6]), "+v"(B[7]) :: "memory")
#define MMA(ai, bj, At, Bt) do { __builtin_amdgcn_s_setprio(1); _Pragma("unroll") for (int m = 0; m < 4; ++m) _Pragma("unroll") for (int n = 0; n < 2; ++n) _Pragma("unroll") for (int k = 0; k < 2; ++k) \
        acc[ai][bj][m][n] = __builtin_amdgcn_mfma_f32_16x16x32_bf16(CAT8(Bt[4 * n + 2 * k], Bt[4 * n + 2 * k + 1]), At[m][k], acc[ai][bj][m][n], 0, 0, 0); __builtin_amdgcn_s_setprio(0); } while (0)
    f32x4 acc[2][2][4][2];
#pragma unroll
    for (int a = 0; a < 2; ++a)
#pragma unroll
        for (int b = 0; b < 2; ++b)
#pragma unroll
            for (int m = 0; m < 4; ++m)
#pragma unroll
                for (int n = 0; n < 2; ++n) acc[a][b][m][n] = (f32x4){0.f, 0.f, 0.f, 0.f};
    bf16x8 At[4][2]; bf16x4 B0[8], B1[8];
    constexpr int nt = D_ / 64;
    STAGE(SB(0, 0), cG, voffB); STAGE(SA(0, 0), cA, voffA); STAGE(SB(0, 1), cU, voffB); STAGE(SA(0, 1), cA + hA, voffA);
    if (wr == 1) BAR();
    WAIT_V(4); BAR();
    STAGE(SB(1, 0), cG + kB, voffB); STAGE(SA(1, 0), cA + kA, voffA); STAGE(SB(1, 1), cU + kB, voffB);
    WAIT_V(6); BAR();
    STAMP();
    for (int t = 0; t < nt; t += 2) {
        const bool more = (t + 2 < nt);
        const char* a1 = cA + (size_t)(t + 1) * kA;
        const char* a2 = a1 + kA; const char* g2 = cG + (size_t)(t + 2) * kB; const char* u2 = cU + (size_t)(t + 2) * kB;
        const char* a3 = a2 + kA; const char* g3 = g2 + kB; const char* u3 = u2 + kB;
        LDB(B0, 0, 0); SCHED(); LDA(At, 0, 0); STAGE(SA(1, 1), a1 + hA, voffA);
        WAIT_L(8); BAR(); WAIT_B(B0); MMA(0, 0, At, B0); BAR(); SCHED();
        LDB(B1, 0, 1); if (more) STAGE(SB(0, 0), g2, voffB);
        BAR(); WAIT_B(B1); MMA(0, 1, At, B1); BAR();
        LDA(At, 0, 1); if (more) STAGE(SA(0, 0), a2, voffA);
        BAR(); WAIT_L(0); MMA(1, 0, At, B0); BAR(); SCHED();
        if (more) { STAGE(SB(0, 1), u2, voffB); WAIT_V(6); } else WAIT_V(0);
        BAR(); MMA(1, 1, At, B1); BAR();
        LDB(B0, 1, 0); SCHED(); LDA(At, 1, 0); if (more) STAGE(SA(0, 1), a2 + hA, voffA);
        WAIT_L(8); BAR(); WAIT_B(B0); MMA(0, 0, At, B0); BAR(); SCHED();
        LDB(B1, 1, 1); if (more) STAGE(SB(1, 0), g3, voffB);
        BAR(); WAIT_B(B1); MMA(0, 1, At, B1); BAR();
        LDA(At, 1, 1); if (more) STAGE(SA(1, 0), a3, voffA);
        BAR(); WAIT_L(0); MMA(1, 0, At, B0); BAR(); SCHED();
        if (more) { STAGE(SB(1, 1), u3, voffB); WAIT_V(6); }
        BAR(); MMA(1, 1, At, B1); BAR();
    }
    STAMP();
    {
        const int wgi = pm * 28 + pn;
        const char* pf = (const char*)wd + (size_t)wgi * 32768 + ldsw + lane * 16;
        const char* pf2 = (const char*)ssmk + (size_t)(wgi < 204 ? wgi : 204) * 20480 + (wid < 4 ? ldsw : 0u) + lane * 16;
        unsigned char* hp = ws + WS_H + ((size_t)(pm * 16 + wr * 4) * 112 + pn * 4 + wc) * 1024 + lane * 16;
#pragma unroll
        for (int ai = 0; ai < 2; ++ai)
#pragma unroll
            for (int m = 0; m < 4; ++m) {
                const f32x4 g0 = acc[ai][0][m][0], g1 = acc[ai][0][m][1], u0 = acc[ai][1][m][0], u1 = acc[ai][1][m][1];
                u32x4 o;
                o.x = gelu_mul2(g0.xy, u0.xy); o.y = gelu_mul2(g0.zw, u0.zw); o.z = gelu_mul2(g1.xy, u1.xy); o.w = gelu_mul2(g1.zw, u1.zw);
                *(u32x4*)(hp + (size_t)(ai * 8 + m) * 112 * 1024) = o;
                if (ai == 0) GLDS16(pf + m * 8192, 8 * HTB + ldsw);
                else if (m < 2) GLDS16(pf2 + ldsw * (wid >= 4) + m * 8192, 8 * HTB + ldsw);
                else if (m == 2 && wgi < 204) GLDS16(pf2 + 16384, 8 * HTB + ldsw);
            }
    }
    STAMP();
    WAIT_V(0);
    STAMP(); STAMP_OUT(0);
    if (wr == 0) BAR();
#undef SA
#undef SB
#undef STAGE
#undef LDA
#undef LDB
#undef MMA
#undef WAIT_B
}

constexpr int CV_UP = 4160, CV_HP = 4640, CV_HB = 4 * CV_UP, CV_CH = CV_HB + 2 * CV_HP;
constexpr int RB = 106496;
constexpr int NT2 = 28;
constexpr int CVF = 4;
constexpr int CV0 = CVF + 2;
constexpr int CVX = CV0 + 12, CVM = CVX + 1, CVS = CVM + 3, CVP = CVS + 2;
constexpr unsigned kSpinCap = 1u << 20;
constexpr int vm_b(int t) { return t + 2 < NT2 ? 2 : 0; }
constexpr int vm_a(int t) { return t + 3 < NT2 ? 2 : 0; }
constexpr int vm_f(int t) { return (t >= 0 && t < CVF) ? 3 : 0; }
constexpr int vm_s(int t) { return t == CVS ? 2 : 0; }
constexpr int vm_all(int t) { return vm_b(t) + vm_a(t) + vm_f(t) + vm_s(t); }
constexpr int vm_wait(int t) { return (t == CVF || t == CVS + 1) ? vm_all(t) : t + 2 < NT2 ? vm_a(t - 1) + vm_f(t - 1) + vm_s(t - 1) + vm_all(t) : 0; }
template <int N> __device__ __forceinline__ void wait_vm() { asm volatile("s_waitcnt vmcnt(%0)" :: "n"(N) : "memory"); }
__device__ constexpr int kSlA[3][12] = {{0,0,0,0,1,1,1,1,2,2,2,2}, {0,0,0,0,1,1,1,2,2,3,3,3}, {3,3,4,4,4,4,5,5,5,6,6,7}};
__device__ constexpr int kSlB[3][12] = {{0,1,2,3,0,1,2,3,0,1,2,3}, {4,5,6,7,4,5,6,4,5,4,3,2}, {0,1,0,1,2,3,0,1,2,0,1,0}};
constexpr int kUlo[3] = {0, 0, 3}, kUhi[3] = {2, 3, 7}, kHlo[3] = {0, 2, 0}, kHhi[3] = {3, 7, 3};

struct K2State {
    f32x4 acc[2][8];
    bf16x8 A[4][2];
    bf16x4 B[16];
    f32x4 cacc[8];
};

__global__ __launch_bounds__(512, 2) void k2_conv_gemm2(const u16* __restrict__ q, const u16* __restrict__ ssmk, const u16* __restrict__ ssmD,
                                                        const u16* __restrict__ wd, unsigned char* __restrict__ ws, u16* __restrict__ out) {
    extern __shared__ __attribute__((aligned(16))) unsigned char smem[];
    LAS unsigned char* lds = (LAS unsigned char*)smem;
    const unsigned lbase = (unsigned)(size_t)lds;
    const int tid = threadIdx.x, wid = __builtin_amdgcn_readfirstlane(tid >> 6), lane = tid & 63, fr = lane & 15, fq = lane >> 4;
    const int bid = blockIdx.x, xg = bid & 7, l = bid >> 3;
    const int z = xg & 1, tm = 8 * ((xg >> 1) & 1) + (l & 7), tn = 4 * (xg >> 2) + (l >> 3);
    const int kh = wid >> 2, wm = wid & 3;
    STAMP_DECL; STAMP();
    unsigned voffB[2];
#pragma unroll
    for (int i = 0; i < 2; ++i) { const int kr = 32 * kh + (wm + 4 * i) * 4 + (lane >> 4), ch = (lane & 15) ^ (2 * (kr & 7)); voffB[i] = (unsigned)(kr * 2048 + ch * 16); }
    const unsigned ldsw = (unsigned)(kh * 8192 + wm * 1024), voffA = (unsigned)lane * 16u;
    const char* cA0 = (const char*)(ws + WS_H) + ((size_t)(tm * 8 + wm * 2) * 112 + z * 56 + kh) * 1024;
    const char* cA1 = cA0 + 112 * 1024;
    const char* cB = (const char*)wd + (size_t)z * 1792 * 2048 + (size_t)tn * 256;
    constexpr size_t kB = (size_t)64 * 2048;
    K2State st; SEG_DECL;
#define STAGEB(t) do { const char* _b = cB + (size_t)(t) * kB; GLDS16(_b + voffB[0], RB + ((t) % 3) * 16384 + ldsw); GLDS16(_b + voffB[1], RB + ((t) % 3) * 16384 + ldsw + 4096); } while (0)
#define LOADA(t) do { asm volatile("global_load_dwordx4 %0, %1, %2" : "=v"(st.A[(t) & 3][0]) : "v"(voffA), "s"(cA0 + (size_t)(t) * 2048) : "memory"); \
        asm volatile("global_load_dwordx4 %0, %1, %2" : "=v"(st.A[(t) & 3][1]) : "v"(voffA), "s"(cA1 + (size_t)(t) * 2048) : "memory"); } while (0)
    const int ch = wid & 3, hf = wid >> 2, d = bid * 4 + ch, CB = ch * CV_CH;
    const unsigned dpair = ((const unsigned*)ssmD)[d >> 1];
    const float dval = (d & 1) ? bf_hi(dpair) : bf_lo(dpair);
    const u32x2 h03 = *(const u32x2*)(ssmk + (size_t)d * S_);
    STAGEB(0); LOADA(0); STAGEB(1); LOADA(1); LOADA(2);
    const char* ur = (const char*)(ws + WS_UR) + (size_t)d * URP * 2 + (3 - 2 * hf) * 2 + lane * 16;
    const auto hrs = __builtin_amdgcn_make_buffer_rsrc((void*)ssmk, 0, D_ * S_ * 2, 0x00020000);
    const int hvo = d * (S_ * 2) + (1 - hf) * 8 + lane * 16;
#define CFILL(i) do { if constexpr ((i) < 8) GLDS16(ur - ((i) >> 2) * 2 + ((i) & 3) * 1024, CB + (2 * hf + ((i) >> 2)) * CV_UP + ((i) & 3) * 1024); \
        else __builtin_amdgcn_raw_ptr_buffer_load_lds(hrs, (LAS void*)(lds + CB + CV_HB + hf * CV_HP + (29 - hf) * 16 + ((i) & 3) * 1024), 16, hvo + ((i) & 3) * 1024, 0, 0, 0); } while (0)
    if (lane < 28) *(LAS u32x4*)(lds + CB + CV_HB + hf * CV_HP + lane * 16) = (u32x4){0u, 0u, 0u, 0u};
    else if (lane < 34) { const int e = lane - 28; *(LAS u32x4*)(lds + CB + (2 * hf + e / 3) * CV_UP + 4096 + (e % 3) * 16) = (u32x4){0u, 0u, 0u, 0u}; }
    else if (lane == 34 && hf == 0) *(LAS u32x4*)(lds + CB + CV_HB + 28 * 16) = (u32x4){0u, 0u, h03.x, h03.y};
    const int tq = fr >> 2, tp4 = lane & 3;
    unsigned boff[8];
    { const unsigned b0 = lbase + RB + (32 * kh + 4 * fq + tq) * 256 + 8 * (tp4 & 1) + (((tp4 >> 1) ^ (2 * ((4 * fq + tq) & 7))) * 16);
#pragma unroll
      for (int n = 0; n < 8; ++n) boff[n] = b0 ^ (32 * n); }
    const unsigned ua = lbase + CB + (fr & 3) * CV_UP + 2 * (2040 - 8 * (fr >> 2) + 8 * fq) - 64 * 59 - 256 * hf;
    const unsigned ha = lbase + CB + CV_HB + (fr & 1) * CV_HP + 2 * (224 + 32 * (fr >> 1) + 8 * fq) - 192 - 256 * hf;
#pragma unroll
    for (int m = 0; m < 2; ++m)
#pragma unroll
        for (int n = 0; n < 8; ++n) st.acc[m][n] = (f32x4){0.f, 0.f, 0.f, 0.f};
#pragma unroll
    for (int n = 0; n < 8; ++n) st.cacc[n] = (f32x4){0.f, 0.f, 0.f, 0.f};
#define RDB(t, n) do { LDS_TR(st.B[2 * (n)], boff[n], ((t) % 3) * 16384); LDS_TR(st.B[2 * (n) + 1], boff[n], ((t) % 3) * 16384 + 4096); } while (0)
#define RD64(dst, vaddr, imm) asm volatile("ds_read_b64 %0, %1 offset:%2" : "=v"(dst) : "v"(vaddr), "i"(imm) : "memory")
    const int sl0 = 32 * (fr >> 1) + 4 * (fr & 1) + 8 * fq;
    const unsigned ypa = lbase + CB + CV_HB + (1 - hf) * CV_HP + lane * 16;
    const unsigned kva = lbase + CB + 3 * CV_UP + 2 * (2044 - 1024 * hf - sl0) - 1536;
    const int twa = 2 * hf * CV_CH + sl0 * 8 + (fr >> 1) * 8 + ch * 2;
    const unsigned tra = lbase + (tid >> 8) * CV_CH + (tid & 255) * 16 + ((tid & 255) >> 4) * 8;
#define RD128(dst, vaddr, imm) asm volatile("ds_read_b128 %0, %1 offset:%2" : "=v"(dst) : "v"(vaddr), "i"(imm) : "memory")
    gu32* flags = (gu32*)(ws + WS_FL);
    const auto yrs = __builtin_amdgcn_make_buffer_rsrc((void*)(ws + WS_Y2 + (size_t)bid * 16384), 0, 16384, 0x00020000);
    wait_vm<6>(); WAIT_L(0); BAR();
    if (kh) BAR();
    STAMP();

    auto trip = [&](auto tc) __attribute__((always_inline)) {
        constexpr int t = decltype(tc)::value;
        if constexpr (t == CV0 || t == CV0 + 6 || t == CVX || t == CVS + 1) STAMP();
        constexpr bool cv = (t >= CV0 && t < CVX);
        constexpr int cc = cv ? (t - CV0) / 3 : 0, sl = cv ? (t - CV0) % 3 : 0;
        constexpr int sgrp = cv ? 1 : (t >= CVX && t <= CVS) ? 2 : 0;
        SEG(sgrp, 0);
        bf16x8 U[8], Hf[8];
        f32x4 yp[2]; u32x2 kvp[4];
        constexpr bool mix = (t == CVM || t == CVM + 1);
        constexpr int mi = mix ? 2 * (t - CVM) : 0;
        if constexpr (mix) {
            RD128(yp[0], ypa, mi * 1024); RD128(yp[1], ypa, mi * 1024 + 1024);
            RD64(kvp[0], kva, 1536 - mi * 512); RD64(kvp[1], kva, 1024 - mi * 512);
        }
        if constexpr (t == CVS) { RD64(kvp[0], tra, 0); RD64(kvp[1], tra, 8); RD64(kvp[2], tra, 2 * CV_CH); RD64(kvp[3], tra, 2 * CV_CH + 8); }
        const char* bsrc = cB + (size_t)(t + 2) * kB;
        RDB(t, 0); RDB(t, 1);
        if constexpr (vm_b(t) != 0) GLDS16(bsrc + voffB[0], RB + ((t + 2) % 3) * 16384 + ldsw);
        SCHED(); RDB(t, 2); RDB(t, 3);
        if constexpr (vm_b(t) != 0) GLDS16(bsrc + voffB[1], RB + ((t + 2) % 3) * 16384 + ldsw + 4096);
        SCHED(); RDB(t, 4); RDB(t, 5);
        if constexpr (vm_a(t) != 0) asm volatile("global_load_dwordx4 %0, %1, %2" : "=v"(st.A[(t + 3) & 3][0]) : "v"(voffA), "s"(cA0 + (size_t)(t + 3) * 2048) : "memory");
        RDB(t, 6); RDB(t, 7);
        if constexpr (vm_a(t) != 0) asm volatile("global_load_dwordx4 %0, %1, %2" : "=v"(st.A[(t + 3) & 3][1]) : "v"(voffA), "s"(cA1 + (size_t)(t + 3) * 2048) : "memory");
        if constexpr (cv) {
#pragma unroll
            for (int a = kUlo[sl]; a <= kUhi[sl]; ++a) RD128(U[a], ua, 64 * (59 - 8 * a - cc));
#pragma unroll
            for (int b = kHlo[sl]; b <= kHhi[sl]; ++b) RD128(Hf[b], ha, 64 * (8 * b - cc + 3));
        }
        if constexpr (vm_f(t) != 0) { CFILL(3 * t); CFILL(3 * t + 1); CFILL(3 * t + 2); }
        if constexpr (t == CVX) {
            if (hf) {
#pragma unroll
                for (int i = 0; i < 4; ++i) { const f32x4 v = st.cacc[i]; st.cacc[i] = st.cacc[4 + i]; st.cacc[4 + i] = v; }
            }
#pragma unroll
            for (int i = 0; i < 4; ++i) *(LAS f32x4*)(lds + CB + CV_HB + hf * CV_HP + lane * 16 + i * 1024) = st.cacc[4 + i];
        }
        if constexpr (t == CVP) { if (tid == 256) __hip_atomic_store(flags + bid * 16, 1u, RLX_AGENT); }
        asm volatile("s_waitcnt lgkmcnt(0)" : "+v"(st.B[0]), "+v"(st.B[1]), "+v"(st.B[2]), "+v"(st.B[3]), "+v"(st.B[4]), "+v"(st.B[5]), "+v"(st.B[6]), "+v"(st.B[7]),
                     "+v"(st.B[8]), "+v"(st.B[9]), "+v"(st.B[10]), "+v"(st.B[11]), "+v"(st.B[12]), "+v"(st.B[13]), "+v"(st.B[14]), "+v"(st.B[15]),
                     "+v"(st.A[t & 3][0]), "+v"(st.A[t & 3][1]) :: "memory");
        if constexpr (cv && sl == 0) asm volatile("" : "+v"(U[0]), "+v"(U[1]), "+v"(U[2]), "+v"(Hf[0]), "+v"(Hf[1]), "+v"(Hf[2]), "+v"(Hf[3]) :: "memory");
        if constexpr (cv && sl == 1) asm volatile("" : "+v"(U[0]), "+v"(U[1]), "+v"(U[2]), "+v"(U[3]), "+v"(Hf[2]), "+v"(Hf[3]), "+v"(Hf[4]), "+v"(Hf[5]), "+v"(Hf[6]), "+v"(Hf[7]) :: "memory");
        if constexpr (cv && sl == 2) asm volatile("" : "+v"(U[3]), "+v"(U[4]), "+v"(U[5]), "+v"(U[6]), "+v"(U[7]), "+v"(Hf[0]), "+v"(Hf[1]), "+v"(Hf[2]), "+v"(Hf[3]) :: "memory");
        if constexpr (mix) asm volatile("" : "+v"(yp[0]), "+v"(yp[1]), "+v"(kvp[0]), "+v"(kvp[1]) :: "memory");
        if constexpr (t == CVS) asm volatile("" : "+v"(kvp[0]), "+v"(kvp[1]), "+v"(kvp[2]), "+v"(kvp[3]) :: "memory");
        SEG(sgrp, 1);
        SCHED(); BAR(); SCHED();
        SEG(sgrp, 2);
        __builtin_amdgcn_s_setprio(1);
#pragma unroll
        for (int n = 0; n < 8; ++n)
#pragma unroll
            for (int m = 0; m < 2; ++m)
                st.acc[m][n] = __builtin_amdgcn_mfma_f32_16x16x32_bf16(CAT8(st.B[2 * n], st.B[2 * n + 1]), st.A[t & 3][m], st.acc[m][n], 0, 0, 0);
        if constexpr (cv) {
#pragma unroll
            for (int i = 0; i < 12; ++i)
                st.cacc[kSlA[sl][i] + kSlB[sl][i]] = __builtin_amdgcn_mfma_f32_16x16x32_bf16(U[kSlA[sl][i]], Hf[kSlB[sl][i]], st.cacc[kSlA[sl][i] + kSlB[sl][i]], 0, 0, 0);
        }
        if constexpr (mix) {
#pragma unroll
            for (int i = 0; i < 2; ++i) {
                const f32x4 y = st.cacc[mi + i] + yp[i];
                const float k3 = bf_lo(kvp[i].x), k2 = bf_hi(kvp[i].x), k1 = bf_lo(kvp[i].y), k0 = bf_hi(kvp[i].y);
                const unsigned p01 = cvt_pk_bf16(rbf(y[0]) + rbf(k0 * dval), rbf(y[1]) + rbf(k1 * dval));
                const unsigned p23 = cvt_pk_bf16(rbf(y[2]) + rbf(k2 * dval), rbf(y[3]) + rbf(k3 * dval));
                LAS u16* tp = (LAS u16*)(lds + twa + (mi >> 1) * CV_CH + i * 2112);
                tp[0] = (u16)p01; tp[4] = (u16)(p01 >> 16); tp[8] = (u16)p23; tp[12] = (u16)(p23 >> 16);
            }
#pragma unroll
            for (int i = 0; i < 16; ++i) { __builtin_amdgcn_sched_group_barrier(0x8, 1, 0); __builtin_amdgcn_sched_group_barrier(0x2, 4, 0); }
        }
        if constexpr (t == CVS) {
            __builtin_amdgcn_raw_buffer_store_b128((u32x4){kvp[0].x, kvp[0].y, kvp[1].x, kvp[1].y}, yrs, tid * 16, 0, 16);
            __builtin_amdgcn_raw_buffer_store_b128((u32x4){kvp[2].x, kvp[2].y, kvp[3].x, kvp[3].y}, yrs, 8192 + tid * 16, 0, 16);
        }
        __builtin_amdgcn_s_setprio(0);
        SCHED();
        if constexpr (mix) WAIT_L(0);
        SEG(sgrp, 3);
        if constexpr (t + 1 < NT2) { wait_vm<vm_wait(t)>(); SEG(sgrp, 4); BAR(); SEG(sgrp, 5); } else { if (!kh) BAR(); }
        SCHED();
    };
#define TRIP4(t) trip(std::integral_constant<int, (t)>{}); trip(std::integral_constant<int, (t) + 1>{}); trip(std::integral_constant<int, (t) + 2>{}); trip(std::integral_constant<int, (t) + 3>{})
    TRIP4(0); TRIP4(4); TRIP4(8); TRIP4(12); TRIP4(16); TRIP4(20); TRIP4(24);
    STAMP();
    {
        if (z) {
#pragma unroll
            for (int n = 0; n < 8; ++n) { const f32x4 v = st.acc[0][n]; st.acc[0][n] = st.acc[1][n]; st.acc[1][n] = v; }
        }
        const auto xs = __builtin_amdgcn_make_buffer_rsrc((void*)(ws + WS_X + (size_t)bid * 65536), 0, 65536, 0x00020000);
#pragma unroll
        for (int n = 0; n < 8; ++n)
            __builtin_amdgcn_raw_buffer_store_b128(__builtin_bit_cast(u32x4, st.acc[1][n]), xs, wid * 8192 + n * 1024 + lane * 16, 0, 16);
        if (kh) {
#pragma unroll
            for (int n = 0; n < 4; ++n) { const f32x4 v = st.acc[0][n]; st.acc[0][n] = st.acc[0][4 + n]; st.acc[0][4 + n] = v; }
        }
#pragma unroll
        for (int n = 0; n < 4; ++n) *(LAS f32x4*)(lds + wid * 4096 + lane * 16 + n * 1024) = st.acc[0][4 + n];
        const int s = tm * 128 + wm * 32 + z * 16 + fr, dcol = tn * 128 + kh * 64 + 4 * fq;
        gu32* fp = flags + ((lane < 16) ? (tn * 32 + kh * 16 + lane) : (256 + (bid ^ 1) * 8 + wm + 4 * (lane & 1))) * 16;
        unsigned fv = __hip_atomic_load(fp, RLX_AGENT);
        u32x2 qv[4];
#pragma unroll
        for (int n = 0; n < 4; ++n) qv[n] = *(const u32x2*)(q + (size_t)s * D_ + dcol + 16 * n);
        const float g = ((const float*)(ws + WS_G))[s];
        STAMP();
        WAIT_V(0);
        if (lane == 0) __hip_atomic_store(flags + (256 + bid * 8 + wid) * 16, 1u, RLX_AGENT);
        const auto xp = __builtin_amdgcn_make_buffer_rsrc((void*)(ws + WS_X + (size_t)(bid ^ 1) * 65536), 0, 65536, 0x00020000);
        const auto y2 = __builtin_amdgcn_make_buffer_rsrc((void*)(ws + WS_Y2), 0, 256 * S_ * 8, 0x00020000);
        unsigned spins = 0;
#pragma nounroll
        while (!__all(lane >= 16 || fv == 1u) && ++spins < kSpinCap) { asm volatile("s_sleep 1" ::: "memory"); fv = __hip_atomic_load(fp, RLX_AGENT); }
        asm volatile("" ::: "memory");
        u32x2 yv[4];
#pragma unroll
        for (int n = 0; n < 4; ++n) yv[n] = __builtin_amdgcn_raw_buffer_load_b64(y2, (((dcol >> 2) + 4 * n) * S_ + s) * 8, 0, 16);
        WAIT_L(0);
        BAR();
        STAMP();
        f32x4 keep[4];
#pragma unroll
        for (int n = 0; n < 4; ++n) keep[n] = st.acc[0][n] + *(const LAS f32x4*)(lds + (wid ^ 4) * 4096 + lane * 16 + n * 1024);
#pragma nounroll
        while (!__all(fv == 1u) && ++spins < kSpinCap) fv = __hip_atomic_load(fp, RLX_AGENT);
        asm volatile("" ::: "memory");
        STAMP();
        u32x4 pr[4], pr2[4];
#pragma unroll
        for (int n = 0; n < 4; ++n) {
            pr[n] = __builtin_amdgcn_raw_buffer_load_b128(xp, wm * 8192 + (4 * kh + n) * 1024 + lane * 16, 0, 16);
            pr2[n] = __builtin_amdgcn_raw_buffer_load_b128(xp, (wm + 4) * 8192 + (4 * kh + n) * 1024 + lane * 16, 0, 16);
        }
        const float omg = rbf(1.0f - g);
#pragma unroll
        for (int n = 0; n < 4; ++n) {
            const f32x4 b = keep[n] + (__builtin_bit_cast(f32x4, pr[n]) + __builtin_bit_cast(f32x4, pr2[n]));
            const float a0 = rbf(bf_lo(qv[n].x) * bf_lo(yv[n].x)), a1 = rbf(bf_hi(qv[n].x) * bf_hi(yv[n].x));
            const float a2 = rbf(bf_lo(qv[n].y) * bf_lo(yv[n].y)), a3 = rbf(bf_hi(qv[n].y) * bf_hi(yv[n].y));
            u32x2 o;
            o.x = cvt_pk_bf16(rbf(g * a0) + rbf(omg * rbf(b[0])), rbf(g * a1) + rbf(omg * rbf(b[1])));
            o.y = cvt_pk_bf16(rbf(g * a2) + rbf(omg * rbf(b[2])), rbf(g * a3) + rbf(omg * rbf(b[3])));
            *(u32x2*)(out + (size_t)s * D_ + dcol + 16 * n) = o;
        }
        STAMP(); WAIT_V(0); STAMP(); STAMP_OUT(1); SEG_OUT();
    }
#undef STAGEB
#undef LOADA
#undef RDB
#undef CFILL
#undef RD128
#undef RD64
#undef TRIP4
}

extern "C" void kernel_launch(void* const* d_in, const int* in_sizes, int n_in, void* d_out, int out_size, void* d_ws, size_t ws_size,
                              hipStream_t stream) {
    const u16* q = (const u16*)d_in[0];
    const u16* k = (const u16*)d_in[1];
    const u16* v = (const u16*)d_in[2];
    const u16* shk = (const u16*)d_in[3];
    const u16* ssmk = (const u16*)d_in[4];
    const u16* ssmD = (const u16*)d_in[5];
    const u16* wg = (const u16*)d_in[7];
    const u16* wu = (const u16*)d_in[8];
    const u16* wd = (const u16*)d_in[9];
    const u16* gw = (const u16*)d_in[10];
    (void)hipFuncSetAttribute((const void*)k1_gemm1_prep, hipFuncAttributeMaxDynamicSharedMemorySize, K1_LDS);
    (void)hipFuncSetAttribute((const void*)k2_conv_gemm2, hipFuncAttributeMaxDynamicSharedMemorySize, K2_LDS);
    k1_gemm1_prep<<<256, 512, K1_LDS, stream>>>(q, k, v, shk, wg, wu, gw, ssmk, wd, (unsigned char*)d_ws);
    k2_conv_gemm2<<<256, 512, K2_LDS, stream>>>(q, ssmk, ssmD, wd, (unsigned char*)d_ws, (u16*)d_out);
}
```

```cpp
#include <hip/hip_runtime.h>
#include <hip/hip_bf16.h>
#include <type_traits>

#define LAS __attribute__((address_space(3)))
#define GAS __attribute__((address_space(1)))
typedef unsigned short u16;
typedef short bf16x8 __attribute__((ext_vector_type(8)));
typedef short bf16x4 __attribute__((ext_vector_type(4)));
typedef float f32x4 __attribute__((ext_vector_type(4)));
typedef float f32x2 __attribute__((ext_vector_type(2)));
typedef unsigned u32x4 __attribute__((ext_vector_type(4)));
typedef unsigned u32x2 __attribute__((ext_vector_type(2)));
typedef GAS unsigned gu32;
typedef GAS unsigned long long gu64;
#define RLX_AGENT __ATOMIC_RELAXED, __HIP_MEMORY_SCOPE_AGENT

constexpr int S_ = 2048, D_ = 1024, F_ = 3584;
constexpr int URP = 2112;
constexpr size_t WS_H = 0;
constexpr size_t WS_UR = WS_H + (size_t)S_ * F_ * 2;
constexpr size_t WS_G = WS_UR + (size_t)D_ * URP * 2;
constexpr size_t WS_Y2 = WS_G + (size_t)S_ * 4;
constexpr size_t WS_X = WS_Y2 + (size_t)256 * S_ * 8;
constexpr size_t WS_FL = WS_X + (size_t)256 * 32768;
constexpr int K1_LDS = 139264, K2_LDS = 163840;

__device__ __forceinline__ unsigned cvt_pk_bf16(float lo, float hi) {
    unsigned r; asm("v_cvt_pk_bf16_f32 %0, %1, %2" : "=v"(r) : "v"(lo), "v"(hi)); return r;
}
__device__ __forceinline__ float bf_lo(unsigned w) { return __uint_as_float(w << 16); }
__device__ __forceinline__ float bf_hi(unsigned w) { return __uint_as_float(w & 0xffff0000u); }
__device__ __forceinline__ float rbf(float x) { return bf_lo(cvt_pk_bf16(x, x)); }

#define GLDS16(gptr, ldsoff) __builtin_amdgcn_global_load_lds((const unsigned*)(gptr), (LAS unsigned*)(lds + (ldsoff)), 16, 0, 0)
#define LDS_B128(off) (*(const LAS bf16x8*)(lds + (off)))
#define LDS_TR(dst, vaddr, imm) asm volatile("ds_read_b64_tr_b16 %0, %1 offset:%2" : "=v"(dst) : "v"(vaddr), "i"(imm) : "memory")
#define CAT8(a, b) __builtin_shufflevector(a, b, 0, 1, 2, 3, 4, 5, 6, 7)
#define WAIT_V(n) asm volatile("s_waitcnt vmcnt(" #n ")" ::: "memory")
#define WAIT_L(n) asm volatile("s_waitcnt lgkmcnt(" #n ")" ::: "memory")
#define BAR() do { asm volatile("" ::: "memory"); __builtin_amdgcn_s_barrier(); asm volatile("" ::: "memory"); } while (0)
#define SCHED() __builtin_amdgcn_sched_barrier(0)

#define STAMP_DECL
#define STAMP()
#define STAMP_OUT(kidx)
#define SEG_DECL
#define SEG(grp, i)
#define SEG_OUT()
__device__ __forceinline__ unsigned gelu_mul2(f32x2 g, f32x2 u) {
    const f32x2 p = __builtin_elementwise_fma(g * g, (f32x2){-0.044715f * 2.3022082f, -0.044715f * 2.3022082f}, (f32x2){-2.3022082f, -2.3022082f});
    const f32x2 y = g * p;
    const f32x2 dd = (f32x2){__builtin_amdgcn_exp2f(fminf(y.x, 60.f)), __builtin_amdgcn_exp2f(fminf(y.y, 60.f))} + 1.0f;
    const float r = __builtin_amdgcn_rcpf(dd.x * dd.y);
    const f32x2 h = (g * u) * ((f32x2){dd.y, dd.x} * r);
    return cvt_pk_bf16(h.x, h.y);
}

__device__ __forceinline__ void prep_block(LAS unsigned char* lds, int j, const u16* x, const u16* kk, const u16* vv, const u16* shk, const u16* gw,
                                           const u16* wg, const u16* wu, unsigned char* ws) {
    const int tid = threadIdx.x, wid = __builtin_amdgcn_readfirstlane(tid >> 6), lane = tid & 63;
    const unsigned ldsw = (unsigned)wid * 1024u;
    STAMP_DECL; STAMP();
    if (tid < 40) *(unsigned*)(ws + WS_FL + (size_t)(j * 40 + tid) * 64) = 0u;
    if (tid >= 256) { const int t = tid - 256; *(u32x4*)(ws + WS_UR + ((size_t)(j * 32 + (t >> 3)) * URP + 2048) * 2 + (t & 7) * 16) = (u32x4){0u, 0u, 0u, 0u}; }
    {
        const char* w = (const char*)((wid < 4) ? wg : wu) + (size_t)j * 14336 + (wid & 3) * 1024 + lane * 16;
        const char* xs = (const char*)x + (size_t)(64 * j + 8 * wid + (lane >> 3)) * 2048 + (lane & 7) * 16;
#pragma unroll 1
        for (int t = 2; t < 16; ++t) {
            const char* p = w + (size_t)t * 64 * 7168;
            GLDS16(xs + t * 128, ldsw); GLDS16(p, ldsw); GLDS16(p + 4096, ldsw); GLDS16(p + 8192, ldsw);
            if ((wid & 3) < 2) GLDS16(p + 12288, ldsw);
            WAIT_V(24);
        }
    }
    WAIT_V(0);
    __syncthreads();
    STAMP();
    const int cg = tid & 127, rg = tid >> 7;
    {
        const int s0 = 64 * j + 16 * rg;
        u32x4 kr[19], vr[16], wr4[4];
#pragma unroll
        for (int i = 0; i < 4; ++i) wr4[i] = ((const u32x4*)(shk + cg * 32))[i];
#pragma unroll
        for (int i = 0; i < 19; ++i) { const int s = s0 - 3 + i; kr[i] = *(const u32x4*)(kk + (size_t)(s < 0 ? 0 : s) * D_ + cg * 8); }
#pragma unroll
        for (int i = 0; i < 16; ++i) vr[i] = *(const u32x4*)(vv + (size_t)(s0 + i) * D_ + cg * 8);
        if (s0 == 0) kr[0] = kr[1] = kr[2] = (u32x4){0u, 0u, 0u, 0u};
#pragma unroll
        for (int cp = 0; cp < 4; ++cp) {
            const f32x2 w0 = {bf_lo(wr4[cp].x), bf_lo(wr4[cp].z)}, w1 = {bf_hi(wr4[cp].x), bf_hi(wr4[cp].z)};
            const f32x2 w2 = {bf_lo(wr4[cp].y), bf_lo(wr4[cp].w)}, w3 = {bf_hi(wr4[cp].y), bf_hi(wr4[cp].w)};
            f32x2 kf[19], o[16];
#pragma unroll
            for (int i = 0; i < 19; ++i) kf[i] = (f32x2){bf_lo(kr[i][cp]), bf_hi(kr[i][cp])};
#pragma unroll
            for (int i = 0; i < 16; ++i) {
                f32x2 a = kf[i] * w3; a = __builtin_elementwise_fma(kf[i + 1], w2, a); a = __builtin_elementwise_fma(kf[i + 2], w1, a); a = __builtin_elementwise_fma(kf[i + 3], w0, a);
                o[i] = a * (f32x2){bf_lo(vr[i][cp]), bf_hi(vr[i][cp])};
            }
#pragma unroll
            for (int hh = 0; hh < 2; ++hh)
#pragma unroll
                for (int e = 0; e < 2; ++e) {
                    u32x4 pk; pk.x = cvt_pk_bf16(o[8 * hh + 7][e], o[8 * hh + 6][e]); pk.y = cvt_pk_bf16(o[8 * hh + 5][e], o[8 * hh + 4][e]);
                    pk.z = cvt_pk_bf16(o[8 * hh + 3][e], o[8 * hh + 2][e]); pk.w = cvt_pk_bf16(o[8 * hh + 1][e], o[8 * hh][e]);
                    const int chunk = 7 - (2 * rg + hh);
                    *(LAS u32x4*)(lds + (cg * 8 + 2 * cp + e) * 128 + ((chunk ^ (cg & 7)) * 16)) = pk;
                }
        }
    }
    STAMP();
    {
        const u32x4 g0 = *(const u32x4*)(gw + lane * 8), g1 = *(const u32x4*)(gw + 512 + lane * 8);
        u32x4 a0[8], a1[8];
#pragma unroll
        for (int r = 0; r < 8; ++r) { const u16* xr = x + (size_t)(64 * j + wid * 8 + r) * D_ + lane * 8; a0[r] = *(const u32x4*)xr; a1[r] = *(const u32x4*)(xr + 512); }
        float acc[8];
#pragma unroll
        for (int r = 0; r < 8; ++r) { float t = 0.f;
#pragma unroll
            for (int i = 0; i < 4; ++i) {
                t += rbf(bf_lo(a0[r][i]) * bf_lo(g0[i])); t += rbf(bf_hi(a0[r][i]) * bf_hi(g0[i]));
                t += rbf(bf_lo(a1[r][i]) * bf_lo(g1[i])); t += rbf(bf_hi(a1[r][i]) * bf_hi(g1[i]));
            }
            acc[r] = t; }
#pragma unroll
        for (int o = 32; o >= 1; o >>= 1)
#pragma unroll
            for (int r = 0; r < 8; ++r) acc[r] += __shfl_xor(acc[r], o);
        float z = acc[0];
#pragma unroll
        for (int r = 1; r < 8; ++r) z = (lane == r) ? acc[r] : z;
        if (lane < 8) ((float*)(ws + WS_G))[64 * j + wid * 8 + lane] = rbf(1.0f / (1.0f + __expf(-rbf(z))));
    }
    STAMP();
    __syncthreads();
    STAMP();
    const size_t xb = (size_t)(1984 - 64 * j) * 2;
#pragma unroll 4
    for (int it = 0; it < 16; ++it) {
        const int ch = it * 64 + (tid >> 3), c = tid & 7;
        const u32x4 t = *(const LAS u32x4*)(lds + ch * 128 + ((c ^ ((ch >> 3) & 7)) * 16));
        *(u32x4*)(ws + WS_UR + (size_t)ch * URP * 2 + xb + c * 16) = t;
    }
    STAMP();
    WAIT_V(0); STAMP(); STAMP_OUT(0);
}

__global__ __launch_bounds__(512, 2) void k1_gemm1_prep(const u16* __restrict__ x, const u16* __restrict__ kk, const u16* __restrict__ vv,
                                                        const u16* __restrict__ shk, const u16* __restrict__ wg, const u16* __restrict__ wu,
                                                        const u16* __restrict__ gw, const u16* __restrict__ ssmk, const u16* __restrict__ wd,
                                                        unsigned char* __restrict__ ws) {
    extern __shared__ __attribute__((aligned(16))) unsigned char smem[];
    LAS unsigned char* lds = (LAS unsigned char*)smem;
    const int xg = blockIdx.x & 7, l = blockIdx.x >> 3;
    if (l >= 28) { prep_block(lds, xg * 4 + (l - 28), x, kk, vv, shk, gw, wg, wu, ws); return; }
    STAMP_DECL; STAMP();
    const int pm = 4 * (xg & 1) + (l & 3), pn = 7 * (xg >> 1) + (l >> 2);
    const int tid = threadIdx.x, wid = __builtin_amdgcn_readfirstlane(tid >> 6), lane = tid & 63, wr = wid >> 2, wc = wid & 3, fr = lane & 15, fq = lane >> 4;
    constexpr int HTB = 16384;
    unsigned voffA[2], voffB[2];
#pragma unroll
    for (int i = 0; i < 2; ++i) {
        const int piece = wid + 8 * i;
        { const int row = piece * 8 + (lane >> 3), ch = (lane & 7) ^ (lane >> 3); voffA[i] = (unsigned)(row * 2048 + ch * 16); }
        { const int kr = piece * 4 + (lane >> 4), fs = (kr & 3) | (((kr >> 3) & 1) << 2), ch = (lane & 15) ^ (2 * fs); voffB[i] = (unsigned)(kr * (F_ * 2) + ch * 16); }
    }
    const unsigned ldsw = (unsigned)wid * 1024u;
    const int aoff0 = ((wr * 64 + fr) >> 3) * 1024 + (fr & 7) * 128 + ((fq ^ (fr & 7)) * 16), aoff1 = aoff0 ^ 64;
    const int tq = (lane & 15) >> 2, tp = lane & 3;
    const unsigned boff0 = (unsigned)(size_t)lds + 4 * HTB + (8 * fq + tq) * 256 + (((4 * wc + (tp >> 1)) ^ (2 * (tq | ((fq & 1) << 2)))) * 16) + 8 * (tp & 1), boff1 = boff0 ^ 32;
    const char* cA = (const char*)x + (size_t)pm * 256 * 2048;
    const char* cG = (const char*)wg + (size_t)pn * 256;
    const char* cU = (const char*)wu + (size_t)pn * 256;
    constexpr size_t kA = 128, kB = (size_t)64 * F_ * 2, hA = (size_t)128 * 2048;
#define SA(b, h) (((b) * 2 + (h)) * HTB)
#define SB(b, h) ((4 + (b) * 2 + (h)) * HTB)
#define STAGE(bufoff, gbase, voff) do { _Pragma("unroll") for (int _i = 0; _i < 2; ++_i) GLDS16((gbase) + (voff)[_i], (bufoff) + ldsw + _i * 8192); } while (0)
#define LDA(dst, b, h) do { _Pragma("unroll") for (int m = 0; m < 4; ++m) { dst[m][0] = LDS_B128(SA(b, h) + aoff0 + m * 2048); dst[m][1] = LDS_B128(SA(b, h) + aoff1 + m * 2048); } } while (0)
#define LDB(dst, b, h) do { \
        LDS_TR(dst[0], boff0, ((b) * 2 + (h)) * HTB); LDS_TR(dst[1], boff0, ((b) * 2 + (h)) * HTB + 1024); \
        LDS_TR(dst[2], boff0, ((b) * 2 + (h)) * HTB + 8192); LDS_TR(dst[3], boff0, ((b) * 2 + (h)) * HTB + 9216); \
        LDS_TR(dst[4], boff1, ((b) * 2 + (h)) * HTB); LDS_TR(dst[5], boff1, ((b) * 2 + (h)) * HTB + 1024); \
        LDS_TR(dst[6], boff1, ((b) * 2 + (h)) * HTB + 8192); LDS_TR(dst[7], boff1, ((b) * 2 + (h)) * HTB + 9216); } while (0)
#define WAIT_B(B) asm volatile("s_waitcnt lgkmcnt(0)" : "+v"(B[0]), "+v"(B[1]), "+v"(B[2]), "+v"(B[3]), "+v"(B[4]), "+v"(B[5]), "+v"(B[6]), "+v"(B[7]) :: "memory")
#define MMA(ai, bj, At, Bt) do { __builtin_amdgcn_s_setprio(1); _Pragma("unroll") for (int m = 0; m < 4; ++m) _Pragma("unroll") for (int n = 0; n < 2; ++n) _Pragma("unroll") for (int k = 0; k < 2; ++k) \
        acc[ai][bj][m][n] = __builtin_amdgcn_mfma_f32_16x16x32_bf16(CAT8(Bt[4 * n + 2 * k], Bt[4 * n + 2 * k + 1]), At[m][k], acc[ai][bj][m][n], 0, 0, 0); __builtin_amdgcn_s_setprio(0); } while (0)
    f32x4 acc[2][2][4][2];
#pragma unroll
    for (int a = 0; a < 2; ++a)
#pragma unroll
        for (int b = 0; b < 2; ++b)
#pragma unroll
            for (int m = 0; m < 4; ++m)
#pragma unroll
                for (int n = 0; n < 2; ++n) acc[a][b][m][n] = (f32x4){0.f, 0.f, 0.f, 0.f};
    bf16x8 At[4][2]; bf16x4 B0[8], B1[8];
    constexpr int nt = D_ / 64;
    STAGE(SB(0, 0), cG, voffB); STAGE(SA(0, 0), cA, voffA); STAGE(SB(0, 1), cU, voffB); STAGE(SA(0, 1), cA + hA, voffA);
    if (wr == 1) BAR();
    WAIT_V(4); BAR();
    STAGE(SB(1, 0), cG + kB, voffB); STAGE(SA(1, 0), cA + kA, voffA); STAGE(SB(1, 1), cU + kB, voffB);
    WAIT_V(6); BAR();
    STAMP();
    for (int t = 0; t < nt; t += 2) {
        const bool more = (t + 2 < nt);
        const char* a1 = cA + (size_t)(t + 1) * kA;
        const char* a2 = a1 + kA; const char* g2 = cG + (size_t)(t + 2) * kB; const char* u2 = cU + (size_t)(t + 2) * kB;
        const char* a3 = a2 + kA; const char* g3 = g2 + kB; const char* u3 = u2 + kB;
        LDB(B0, 0, 0); SCHED(); LDA(At, 0, 0); STAGE(SA(1, 1), a1 + hA, voffA);
        WAIT_L(8); BAR(); WAIT_B(B0); MMA(0, 0, At, B0); BAR(); SCHED();
        LDB(B1, 0, 1); if (more) STAGE(SB(0, 0), g2, voffB);
        BAR(); WAIT_B(B1); MMA(0, 1, At, B1); BAR();
        LDA(At, 0, 1); if (more) STAGE(SA(0, 0), a2, voffA);
        BAR(); WAIT_L(0); MMA(1, 0, At, B0); BAR(); SCHED();
        if (more) { STAGE(SB(0, 1), u2, voffB); WAIT_V(6); } else WAIT_V(0);
        BAR(); MMA(1, 1, At, B1); BAR();
        LDB(B0, 1, 0); SCHED(); LDA(At, 1, 0); if (more) STAGE(SA(0, 1), a2 + hA, voffA);
        WAIT_L(8); BAR(); WAIT_B(B0); MMA(0, 0, At, B0); BAR(); SCHED();
        LDB(B1, 1, 1); if (more) STAGE(SB(1, 0), g3, voffB);
        BAR(); WAIT_B(B1); MMA(0, 1, At, B1); BAR();
        LDA(At, 1, 1); if (more) STAGE(SA(1, 0), a3, voffA);
        BAR(); WAIT_L(0); MMA(1, 0, At, B0); BAR(); SCHED();
        if (more) { STAGE(SB(1, 1), u3, voffB); WAIT_V(6); }
        BAR(); MMA(1, 1, At, B1); BAR();
    }
    STAMP();
    {
        const int wgi = pm * 28 + pn;
        const char* pf = (const char*)wd + (size_t)wgi * 32768 + ldsw + lane * 16;
        const char* pf2 = (const char*)ssmk + (size_t)(wgi < 204 ? wgi : 204) * 20480 + (wid < 4 ? ldsw : 0u) + lane * 16;
        unsigned char* hp = ws + WS_H + ((size_t)(pm * 16 + wr * 4) * 112 + pn * 4 + wc) * 1024 + lane * 16;
#pragma unroll
        for (int ai = 0; ai < 2; ++ai)
#pragma unroll
            for (int m = 0; m < 4; ++m) {
                const f32x4 g0 = acc[ai][0][m][0], g1 = acc[ai][0][m][1], u0 = acc[ai][1][m][0], u1 = acc[ai][1][m][1];
                u32x4 o;
                o.x = gelu_mul2(g0.xy, u0.xy); o.y = gelu_mul2(g0.zw, u0.zw); o.z = gelu_mul2(g1.xy, u1.xy); o.w = gelu_mul2(g1.zw, u1.zw);
                *(u32x4*)(hp + (size_t)(ai * 8 + m) * 112 * 1024) = o;
                if (ai == 0) GLDS16(pf + m * 8192, 8 * HTB + ldsw);
                else if (m < 2) GLDS16(pf2 + ldsw * (wid >= 4) + m * 8192, 8 * HTB + ldsw);
                else if (m == 2 && wgi < 204) GLDS16(pf2 + 16384, 8 * HTB + ldsw);
            }
    }
    STAMP();
    WAIT_V(0);
    STAMP(); STAMP_OUT(0);
    if (wr == 0) BAR();
#undef SA
#undef SB
#undef STAGE
#undef LDA
#undef LDB
#undef MMA
#undef WAIT_B
}

constexpr int CV_UP = 4160, CV_HP = 4640, CV_HB = 4 * CV_UP, CV_CH = CV_HB + 2 * CV_HP;
constexpr int RB = 106496;
constexpr int NT2 = 28;
constexpr int CVF = 4;
constexpr int CV0 = CVF + 2;
constexpr int CVX = CV0 + 12, CVM = CVX + 1, CVS = CVM + 3, CVP = CVS + 2;
constexpr unsigned kSpinCap = 1u << 20;
constexpr int vm_b(int t) { return t + 2 < NT2 ? 2 : 0; }
constexpr int vm_a(int t) { return t + 3 < NT2 ? 2 : 0; }
constexpr int vm_f(int t) { return (t >= 0 && t < CVF) ? 3 : 0; }
constexpr int vm_s(int t) { return t == CVS ? 2 : 0; }
constexpr int vm_all(int t) { return vm_b(t) + vm_a(t) + vm_f(t) + vm_s(t); }
constexpr int vm_wait(int t) { return (t == CVF || t == CVS + 1) ? vm_all(t) : t + 2 < NT2 ? vm_a(t - 1) + vm_f(t - 1) + vm_s(t - 1) + vm_all(t) : 0; }
template <int N> __device__ __forceinline__ void wait_vm() { asm volatile("s_waitcnt vmcnt(%0)" :: "n"(N) : "memory"); }
__device__ constexpr int kSlA[3][12] = {{0,0,0,0,1,1,1,1,2,2,2,2}, {0,0,0,0,1,1,1,2,2,3,3,3}, {3,3,4,4,4,4,5,5,5,6,6,7}};
__device__ constexpr int kSlB[3][12] = {{0,1,2,3,0,1,2,3,0,1,2,3}, {4,5,6,7,4,5,6,4,5,4,3,2}, {0,1,0,1,2,3,0,1,2,0,1,0}};
constexpr int kUlo[3] = {0, 0, 3}, kUhi[3] = {2, 3, 7}, kHlo[3] = {0, 2, 0}, kHhi[3] = {3, 7, 3};

struct K2State {
    f32x4 acc[2][8];
    bf16x8 A[4][2];
    bf16x4 B[16];
    f32x4 cacc[8];
};

__global__ __launch_bounds__(512, 2) void k2_conv_gemm2(const u16* __restrict__ q, const u16* __restrict__ ssmk, const u16* __restrict__ ssmD,
                                                        const u16* __restrict__ wd, unsigned char* __restrict__ ws, u16* __restrict__ out) {
    extern __shared__ __attribute__((aligned(16))) unsigned char smem[];
    LAS unsigned char* lds = (LAS unsigned char*)smem;
    const unsigned lbase = (unsigned)(size_t)lds;
    const int tid = threadIdx.x, wid = __builtin_amdgcn_readfirstlane(tid >> 6), lane = tid & 63, fr = lane & 15, fq = lane >> 4;
    const int bid = blockIdx.x, xg = bid & 7, l = bid >> 3;
    const int z = xg & 1, tm = 8 * ((xg >> 1) & 1) + (l & 7), tn = 4 * (xg >> 2) + (l >> 3);
    const int kh = wid >> 2, wm = wid & 3;
    STAMP_DECL; STAMP();
    unsigned voffB[2];
#pragma unroll
    for (int i = 0; i < 2; ++i) { const int kr = 32 * kh + (wm + 4 * i) * 4 + (lane >> 4), ch = (lane & 15) ^ (2 * (kr & 7)); voffB[i] = (unsigned)(kr * 2048 + ch * 16); }
    const unsigned ldsw = (unsigned)(kh * 8192 + wm * 1024), voffA = (unsigned)lane * 16u;
    const char* cA0 = (const char*)(ws + WS_H) + ((size_t)(tm * 8 + wm * 2) * 112 + z * 56 + kh) * 1024;
    const char* cA1 = cA0 + 112 * 1024;
    const char* cB = (const char*)wd + (size_t)z * 1792 * 2048 + (size_t)tn * 256;
    constexpr size_t kB = (size_t)64 * 2048;
    K2State st; SEG_DECL;
#define STAGEB(t) do { const char* _b = cB + (size_t)(t) * kB; GLDS16(_b + voffB[0], RB + ((t) % 3) * 16384 + ldsw); GLDS16(_b + voffB[1], RB + ((t) % 3) * 16384 + ldsw + 4096); } while (0)
#define LOADA(t) do { asm volatile("global_load_dwordx4 %0, %1, %2" : "=v"(st.A[(t) & 3][0]) : "v"(voffA), "s"(cA0 + (size_t)(t) * 2048) : "memory"); \
        asm volatile("global_load_dwordx4 %0, %1, %2" : "=v"(st.A[(t) & 3][1]) : "v"(voffA), "s"(cA1 + (size_t)(t) * 2048) : "memory"); } while (0)
    const int ch = wid & 3, hf = wid >> 2, d = bid * 4 + ch, CB = ch * CV_CH;
    const unsigned dpair = ((const unsigned*)ssmD)[d >> 1];
    const float dval = (d & 1) ? bf_hi(dpair) : bf_lo(dpair);
    const u32x2 h03 = *(const u32x2*)(ssmk + (size_t)d * S_);
    STAGEB(0); LOADA(0); STAGEB(1); LOADA(1); LOADA(2);
    const char* ur = (const char*)(ws + WS_UR) + (size_t)d * URP * 2 + (3 - 2 * hf) * 2 + lane * 16;
    const auto hrs = __builtin_amdgcn_make_buffer_rsrc((void*)ssmk, 0, D_ * S_ * 2, 0x00020000);
    const int hvo = d * (S_ * 2) + (1 - hf) * 8 + lane * 16;
#define CFILL(i) do { if constexpr ((i) < 8) GLDS16(ur - ((i) >> 2) * 2 + ((i) & 3) * 1024, CB + (2 * hf + ((i) >> 2)) * CV_UP + ((i) & 3) * 1024); \
        else __builtin_amdgcn_raw_ptr_buffer_load_lds(hrs, (LAS void*)(lds + CB + CV_HB + hf * CV_HP + (29 - hf) * 16 + ((i) & 3) * 1024), 16, hvo + ((i) & 3) * 1024, 0, 0, 0); } while (0)
    if (lane < 28) *(LAS u32x4*)(lds + CB + CV_HB + hf * CV_HP + lane * 16) = (u32x4){0u, 0u, 0u, 0u};
    else if (lane < 34) { const int e = lane - 28; *(LAS u32x4*)(lds + CB + (2 * hf + e / 3) * CV_UP + 4096 + (e % 3) * 16) = (u32x4){0u, 0u, 0u, 0u}; }
    else if (lane == 34 && hf == 0) *(LAS u32x4*)(lds + CB + CV_HB + 28 * 16) = (u32x4){0u, 0u, h03.x, h03.y};
    const int tq = fr >> 2, tp4 = lane & 3;
    unsigned boff[8];
    { const unsigned b0 = lbase + RB + (32 * kh + 4 * fq + tq) * 256 + 8 * (tp4 & 1) + (((tp4 >> 1) ^ (2 * ((4 * fq + tq) & 7))) * 16);
#pragma unroll
      for (int n = 0; n < 8; ++n) boff[n] = b0 ^ (32 * n); }
    const unsigned ua = lbase + CB + (fr & 3) * CV_UP + 2 * (2040 - 8 * (fr >> 2) + 8 * fq) - 64 * 59 - 256 * hf;
    const unsigned ha = lbase + CB + CV_HB + (fr & 1) * CV_HP + 2 * (224 + 32 * (fr >> 1) + 8 * fq) - 192 - 256 * hf;
#pragma unroll
    for (int m = 0; m < 2; ++m)
#pragma unroll
        for (int n = 0; n < 8; ++n) st.acc[m][n] = (f32x4){0.f, 0.f, 0.f, 0.f};
#pragma unroll
    for (int n = 0; n < 8; ++n) st.cacc[n] = (f32x4){0.f, 0.f, 0.f, 0.f};
#define RDB(t, n) do { LDS_TR(st.B[2 * (n)], boff[n], ((t) % 3) * 16384); LDS_TR(st.B[2 * (n) + 1], boff[n], ((t) % 3) * 16384 + 4096); } while (0)
#define RD64(dst, vaddr, imm) asm volatile("ds_read_b64 %0, %1 offset:%2" : "=v"(dst) : "v"(vaddr), "i"(imm) : "memory")
    const int sl0 = 32 * (fr >> 1) + 4 * (fr & 1) + 8 * fq;
    const unsigned ypa = lbase + CB + CV_HB + (1 - hf) * CV_HP + lane * 16;
    const unsigned kva = lbase + CB + 3 * CV_UP + 2 * (2044 - 1024 * hf - sl0) - 1536;
    const int twa = 2 * hf * CV_CH + sl0 * 8 + (fr >> 1) * 8 + ch * 2;
    const unsigned tra = lbase + (tid >> 8) * CV_CH + (tid & 255) * 16 + ((tid & 255) >> 4) * 8;
#define RD128(dst, vaddr, imm) asm volatile("ds_read_b128 %0, %1 offset:%2" : "=v"(dst) : "v"(vaddr), "i"(imm) : "memory")
    gu32* flags = (gu32*)(ws + WS_FL);
    const auto yrs = __builtin_amdgcn_make_buffer_rsrc((void*)(ws + WS_Y2 + (size_t)bid * 16384), 0, 16384, 0x00020000);
    wait_vm<6>(); WAIT_L(0); BAR();
    if (kh) BAR();
    STAMP();

    auto trip = [&](auto tc) __attribute__((always_inline)) {
        constexpr int t = decltype(tc)::value;
        if constexpr (t == CV0 || t == CV0 + 6 || t == CVX || t == CVS + 1) STAMP();
        constexpr bool cv = (t >= CV0 && t < CVX);
        constexpr int cc = cv ? (t - CV0) / 3 : 0, sl = cv ? (t - CV0) % 3 : 0;
        constexpr int sgrp = cv ? 1 : (t >= CVX && t <= CVS) ? 2 : 0;
        SEG(sgrp, 0);
        bf16x8 U[8], Hf[8];
        f32x4 yp[2]; u32x2 kvp[4];
        constexpr bool mix = (t == CVM || t == CVM + 1);
        constexpr int mi = mix ? 2 * (t - CVM) : 0;
        if constexpr (mix) {
            RD128(yp[0], ypa, mi * 1024); RD128(yp[1], ypa, mi * 1024 + 1024);
            RD64(kvp[0], kva, 1536 - mi * 512); RD64(kvp[1], kva, 1024 - mi * 512);
        }
        if constexpr (t == CVS) { RD64(kvp[0], tra, 0); RD64(kvp[1], tra, 8); RD64(kvp[2], tra, 2 * CV_CH); RD64(kvp[3], tra, 2 * CV_CH + 8); }
        const char* bsrc = cB + (size_t)(t + 2) * kB;
        RDB(t, 0); RDB(t, 1);
        if constexpr (vm_b(t) != 0) GLDS16(bsrc + voffB[0], RB + ((t + 2) % 3) * 16384 + ldsw);
        SCHED(); RDB(t, 2); RDB(t, 3);
        if constexpr (vm_b(t) != 0) GLDS16(bsrc + voffB[1], RB + ((t + 2) % 3) * 16384 + ldsw + 4096);
        SCHED(); RDB(t, 4); RDB(t, 5);
        if constexpr (vm_a(t) != 0) asm volatile("global_load_dwordx4 %0, %1, %2" : "=v"(st.A[(t + 3) & 3][0]) : "v"(voffA), "s"(cA0 + (size_t)(t + 3) * 2048) : "memory");
        RDB(t, 6); RDB(t, 7);
        if constexpr (vm_a(t) != 0) asm volatile("global_load_dwordx4 %0, %1, %2" : "=v"(st.A[(t + 3) & 3][1]) : "v"(voffA), "s"(cA1 + (size_t)(t + 3) * 2048) : "memory");
        if constexpr (cv) {
#pragma unroll
            for (int a = kUlo[sl]; a <= kUhi[sl]; ++a) RD128(U[a], ua, 64 * (59 - 8 * a - cc));
#pragma unroll
            for (int b = kHlo[sl]; b <= kHhi[sl]; ++b) RD128(Hf[b], ha, 64 * (8 * b - cc + 3));
        }
        if constexpr (vm_f(t) != 0) { CFILL(3 * t); CFILL(3 * t + 1); CFILL(3 * t + 2); }
        if constexpr (t == CVX) {
            if (hf) {
#pragma unroll
                for (int i = 0; i < 4; ++i) { const f32x4 v = st.cacc[i]; st.cacc[i] = st.cacc[4 + i]; st.cacc[4 + i] = v; }
            }
#pragma unroll
            for (int i = 0; i < 4; ++i) *(LAS f32x4*)(lds + CB + CV_HB + hf * CV_HP + lane * 16 + i * 1024) = st.cacc[4 + i];
        }
        if constexpr (t == CVP) { if (tid == 256) __hip_atomic_store(flags + bid * 16, 1u, RLX_AGENT); }
        asm volatile("s_waitcnt lgkmcnt(0)" : "+v"(st.B[0]), "+v"(st.B[1]), "+v"(st.B[2]), "+v"(st.B[3]), "+v"(st.B[4]), "+v"(st.B[5]), "+v"(st.B[6]), "+v"(st.B[7]),
                     "+v"(st.B[8]), "+v"(st.B[9]), "+v"(st.B[10]), "+v"(st.B[11]), "+v"(st.B[12]), "+v"(st.B[13]), "+v"(st.B[14]), "+v"(st.B[15]),
                     "+v"(st.A[t & 3][0]), "+v"(st.A[t & 3][1]) :: "memory");
        if constexpr (cv && sl == 0) asm volatile("" : "+v"(U[0]), "+v"(U[1]), "+v"(U[2]), "+v"(Hf[0]), "+v"(Hf[1]), "+v"(Hf[2]), "+v"(Hf[3]) :: "memory");
        if constexpr (cv && sl == 1) asm volatile("" : "+v"(U[0]), "+v"(U[1]), "+v"(U[2]), "+v"(U[3]), "+v"(Hf[2]), "+v"(Hf[3]), "+v"(Hf[4]), "+v"(Hf[5]), "+v"(Hf[6]), "+v"(Hf[7]) :: "memory");
        if constexpr (cv && sl == 2) asm volatile("" : "+v"(U[3]), "+v"(U[4]), "+v"(U[5]), "+v"(U[6]), "+v"(U[7]), "+v"(Hf[0]), "+v"(Hf[1]), "+v"(Hf[2]), "+v"(Hf[3]) :: "memory");
        if constexpr (mix) asm volatile("" : "+v"(yp[0]), "+v"(yp[1]), "+v"(kvp[0]), "+v"(kvp[1]) :: "memory");
        if constexpr (t == CVS) asm volatile("" : "+v"(kvp[0]), "+v"(kvp[1]), "+v"(kvp[2]), "+v"(kvp[3]) :: "memory");
        SEG(sgrp, 1);
        SCHED(); BAR(); SCHED();
        SEG(sgrp, 2);
        __builtin_amdgcn_s_setprio(1);
#pragma unroll
        for (int n = 0; n < 8; ++n)
#pragma unroll
            for (int m = 0; m < 2; ++m)
                st.acc[m][n] = __builtin_amdgcn_mfma_f32_16x16x32_bf16(CAT8(st.B[2 * n], st.B[2 * n + 1]), st.A[t & 3][m], st.acc[m][n], 0, 0, 0);
        if constexpr (cv) {
#pragma unroll
            for (int i = 0; i < 12; ++i)
                st.cacc[kSlA[sl][i] + kSlB[sl][i]] = __builtin_amdgcn_mfma_f32_16x16x32_bf16(U[kSlA[sl][i]], Hf[kSlB[sl][i]], st.cacc[kSlA[sl][i] + kSlB[sl][i]], 0, 0, 0);
        }
        if constexpr (mix) {
#pragma unroll
            for (int i = 0; i < 2; ++i) {
                const f32x4 y = st.cacc[mi + i] + yp[i];
                const float k3 = bf_lo(kvp[i].x), k2 = bf_hi(kvp[i].x), k1 = bf_lo(kvp[i].y), k0 = bf_hi(kvp[i].y);
                const unsigned p01 = cvt_pk_bf16(fmaf(k0, dval, y[0]), fmaf(k1, dval, y[1]));
                const unsigned p23 = cvt_pk_bf16(fmaf(k2, dval, y[2]), fmaf(k3, dval, y[3]));
                LAS u16* tp = (LAS u16*)(lds + twa + (mi >> 1) * CV_CH + i * 2112);
                tp[0] = (u16)p01; tp[4] = (u16)(p01 >> 16); tp[8] = (u16)p23; tp[12] = (u16)(p23 >> 16);
            }
#pragma unroll
            for (int i = 0; i < 16; ++i) { __builtin_amdgcn_sched_group_barrier(0x8, 1, 0); __builtin_amdgcn_sched_group_barrier(0x2, 2, 0); }
        }
        if constexpr (t == CVS) {
            __builtin_amdgcn_raw_buffer_store_b128((u32x4){kvp[0].x, kvp[0].y, kvp[1].x, kvp[1].y}, yrs, tid * 16, 0, 16);
            __builtin_amdgcn_raw_buffer_store_b128((u32x4){kvp[2].x, kvp[2].y, kvp[3].x, kvp[3].y}, yrs, 8192 + tid * 16, 0, 16);
        }
        __builtin_amdgcn_s_setprio(0);
        SCHED();
        if constexpr (mix) WAIT_L(0);
        SEG(sgrp, 3);
        if constexpr (t + 1 < NT2) { wait_vm<vm_wait(t)>(); SEG(sgrp, 4); BAR(); SEG(sgrp, 5); } else { if (!kh) BAR(); }
        SCHED();
    };
#define TRIP4(t) trip(std::integral_constant<int, (t)>{}); trip(std::integral_constant<int, (t) + 1>{}); trip(std::integral_constant<int, (t) + 2>{}); trip(std::integral_constant<int, (t) + 3>{})
    TRIP4(0); TRIP4(4); TRIP4(8); TRIP4(12); TRIP4(16); TRIP4(20); TRIP4(24);
    STAMP();
    if (z != kh) {
#pragma unroll
        for (int n = 0; n < 8; ++n) { const f32x4 v = st.acc[0][n]; st.acc[0][n] = st.acc[1][n]; st.acc[1][n] = v; }
    }
#pragma unroll
    for (int n = 0; n < 8; ++n) *(LAS f32x4*)(lds + wid * 8192 + lane * 16 + n * 1024) = st.acc[1][n];
    if (kh) {
        WAIT_L(0); BAR();
        const auto xs = __builtin_amdgcn_make_buffer_rsrc((void*)(ws + WS_X + (size_t)bid * 32768), 0, 32768, 0x00020000);
#pragma unroll
        for (int n = 0; n < 8; ++n)
            __builtin_amdgcn_raw_buffer_store_b128(__builtin_bit_cast(u32x4, st.acc[0][n] + *(const LAS f32x4*)(lds + wm * 8192 + lane * 16 + n * 1024)), xs,
                                                   wm * 8192 + n * 1024 + lane * 16, 0, 16);
        STAMP();
        WAIT_V(0);
        if (lane == 0) __hip_atomic_store(flags + (256 + bid * 4 + wm) * 16, 1u, RLX_AGENT);
        STAMP(); STAMP_OUT(1); SEG_OUT();
    } else {
        const int s = tm * 128 + wm * 32 + z * 16 + fr, dcol = tn * 128 + 4 * fq;
        gu32* fp = flags + ((lane < 32) ? (tn * 32 + lane) : (256 + (bid ^ 1) * 4 + wm)) * 16;
        unsigned fv = __hip_atomic_load(fp, RLX_AGENT);
        u32x2 qv[8];
#pragma unroll
        for (int n = 0; n < 8; ++n) qv[n] = *(const u32x2*)(q + (size_t)s * D_ + dcol + 16 * n);
        const float g = ((const float*)(ws + WS_G))[s];
        const auto xp = __builtin_amdgcn_make_buffer_rsrc((void*)(ws + WS_X + (size_t)(bid ^ 1) * 32768), 0, 32768, 0x00020000);
        const auto y2 = __builtin_amdgcn_make_buffer_rsrc((void*)(ws + WS_Y2), 0, 256 * S_ * 8, 0x00020000);
        WAIT_L(0); BAR();
        unsigned spins = 0;
#pragma nounroll
        while (!__all(lane >= 32 || fv == 1u) && ++spins < kSpinCap) { asm volatile("s_sleep 1" ::: "memory"); fv = __hip_atomic_load(fp, RLX_AGENT); }
        asm volatile("" ::: "memory");
        u32x2 yv[8];
#pragma unroll
        for (int n = 0; n < 8; ++n) yv[n] = __builtin_amdgcn_raw_buffer_load_b64(y2, (((dcol >> 2) + 4 * n) * S_ + s) * 8, 0, 16);
#pragma unroll
        for (int n = 0; n < 8; ++n) st.acc[0][n] += *(const LAS f32x4*)(lds + (wm + 4) * 8192 + lane * 16 + n * 1024);
        f32x4 ga[8];
#pragma unroll
        for (int n = 0; n < 8; ++n) {
            ga[n][0] = rbf(g * rbf(bf_lo(qv[n].x) * bf_lo(yv[n].x))); ga[n][1] = rbf(g * rbf(bf_hi(qv[n].x) * bf_hi(yv[n].x)));
            ga[n][2] = rbf(g * rbf(bf_lo(qv[n].y) * bf_lo(yv[n].y))); ga[n][3] = rbf(g * rbf(bf_hi(qv[n].y) * bf_hi(yv[n].y)));
        }
        STAMP();
#pragma nounroll
        while (!__all(fv == 1u) && ++spins < kSpinCap) fv = __hip_atomic_load(fp, RLX_AGENT);
        asm volatile("" ::: "memory");
        STAMP();
        u32x4 pr[8];
#pragma unroll
        for (int n = 0; n < 8; ++n) pr[n] = __builtin_amdgcn_raw_buffer_load_b128(xp, wm * 8192 + n * 1024 + lane * 16, 0, 16);
        const float omg = rbf(1.0f - g);
#pragma unroll
        for (int n = 0; n < 8; ++n) {
            const f32x4 b = st.acc[0][n] + __builtin_bit_cast(f32x4, pr[n]);
            u32x2 o;
            o.x = cvt_pk_bf16(ga[n][0] + rbf(omg * rbf(b[0])), ga[n][1] + rbf(omg * rbf(b[1])));
            o.y = cvt_pk_bf16(ga[n][2] + rbf(omg * rbf(b[2])), ga[n][3] + rbf(omg * rbf(b[3])));
            *(u32x2*)(out + (size_t)s * D_ + dcol + 16 * n) = o;
        }
        STAMP(); WAIT_V(0); STAMP(); STAMP_OUT(1); SEG_OUT();
    }
#undef STAGEB
#undef LOADA
#undef RDB
#undef CFILL
#undef RD128
#undef RD64
#undef TRIP4
}

extern "C" void kernel_launch(void* const* d_in, const int* in_sizes, int n_in, void* d_out, int out_size, void* d_ws, size_t ws_size,
                              hipStream_t stream) {
    const u16* q = (const u16*)d_in[0];
    const u16* k = (const u16*)d_in[1];
    const u16* v = (const u16*)d_in[2];
    const u16* shk = (const u16*)d_in[3];
    const u16* ssmk = (const u16*)d_in[4];
    const u16* ssmD = (const u16*)d_in[5];
    const u16* wg = (const u16*)d_in[7];
    const u16* wu = (const u16*)d_in[8];
    const u16* wd = (const u16*)d_in[9];
    const u16* gw = (const u16*)d_in[10];
    (void)hipFuncSetAttribute((const void*)k1_gemm1_prep, hipFuncAttributeMaxDynamicSharedMemorySize, K1_LDS);
    (void)hipFuncSetAttribute((const void*)k2_conv_gemm2, hipFuncAttributeMaxDynamicSharedMemorySize, K2_LDS);
    k1_gemm1_prep<<<256, 512, K1_LDS, stream>>>(q, k, v, shk, wg, wu, gw, ssmk, wd, (unsigned char*)d_ws);
    k2_conv_gemm2<<<256, 512, K2_LDS, stream>>>(q, ssmk, ssmD, wd, (unsigned char*)d_ws, (u16*)d_out);
}
```

```cpp
#include <hip/hip_runtime.h>
#include <hip/hip_bf16.h>
#include <type_traits>

#define LAS __attribute__((address_space(3)))
#define GAS __attribute__((address_space(1)))
typedef unsigned short u16;
typedef short bf16x8 __attribute__((ext_vector_type(8)));
typedef short bf16x4 __attribute__((ext_vector_type(4)));
typedef float f32x4 __attribute__((ext_vector_type(4)));
typedef float f32x2 __attribute__((ext_vector_type(2)));
typedef unsigned u32x4 __attribute__((ext_vector_type(4)));
typedef unsigned u32x2 __attribute__((ext_vector_type(2)));
typedef GAS unsigned gu32;
typedef GAS unsigned long long gu64;
#define RLX_AGENT __ATOMIC_RELAXED, __HIP_MEMORY_SCOPE_AGENT

constexpr int S_ = 2048, D_ = 1024, F_ = 3584;
constexpr int URP = 2112;
constexpr size_t WS_H = 0;
constexpr size_t WS_UR = WS_H + (size_t)S_ * F_ * 2;
constexpr size_t WS_G = WS_UR + (size_t)D_ * URP * 2;
constexpr size_t WS_Y2 = WS_G + (size_t)S_ * 4;
constexpr size_t WS_X = WS_Y2 + (size_t)256 * S_ * 8;
constexpr size_t WS_FL = WS_X + (size_t)256 * 32768;
#define HST_AUX 16
constexpr int K1_LDS = 139264, K2_LDS = 163840;

__device__ __forceinline__ unsigned cvt_pk_bf16(float lo, float hi) {
    unsigned r; asm("v_cvt_pk_bf16_f32 %0, %1, %2" : "=v"(r) : "v"(lo), "v"(hi)); return r;
}
__device__ __forceinline__ float bf_lo(unsigned w) { return __uint_as_float(w << 16); }
__device__ __forceinline__ float bf_hi(unsigned w) { return __uint_as_float(w & 0xffff0000u); }
__device__ __forceinline__ float rbf(float x) { return bf_lo(cvt_pk_bf16(x, x)); }

#define GLDS16(gptr, ldsoff) __builtin_amdgcn_global_load_lds((const unsigned*)(gptr), (LAS unsigned*)(lds + (ldsoff)), 16, 0, 0)
#define LDS_B128(off) (*(const LAS bf16x8*)(lds + (off)))
#define LDS_TR(dst, vaddr, imm) asm volatile("ds_read_b64_tr_b16 %0, %1 offset:%2" : "=v"(dst) : "v"(vaddr), "i"(imm) : "memory")
#define CAT8(a, b) __builtin_shufflevector(a, b, 0, 1, 2, 3, 4, 5, 6, 7)
#define WAIT_V(n) asm volatile("s_waitcnt vmcnt(" #n ")" ::: "memory")
#define WAIT_L(n) asm volatile("s_waitcnt lgkmcnt(" #n ")" ::: "memory")
#define BAR() do { asm volatile("" ::: "memory"); __builtin_amdgcn_s_barrier(); asm volatile("" ::: "memory"); } while (0)
#define SCHED() __builtin_amdgcn_sched_barrier(0)

#define STAMP_DECL
#define STAMP()
#define STAMP_OUT(kidx)
#define SEG_DECL
#define SEG(grp, i)
#define SEG_OUT()
__device__ __forceinline__ unsigned gelu_mul2(f32x2 g, f32x2 u) {
    const f32x2 p = __builtin_elementwise_fma(g * g, (f32x2){-0.044715f * 2.3022082f, -0.044715f * 2.3022082f}, (f32x2){-2.3022082f, -2.3022082f});
    const f32x2 y = g * p;
    const f32x2 dd = (f32x2){__builtin_amdgcn_exp2f(fminf(y.x, 60.f)), __builtin_amdgcn_exp2f(fminf(y.y, 60.f))} + 1.0f;
    const float r = __builtin_amdgcn_rcpf(dd.x * dd.y);
    const f32x2 h = (g * u) * ((f32x2){dd.y, dd.x} * r);
    return cvt_pk_bf16(h.x, h.y);
}

__device__ __forceinline__ void prep_block(LAS unsigned char* lds, int j, const u16* x, const u16* kk, const u16* vv, const u16* shk, const u16* gw,
                                           const u16* wg, const u16* wu, unsigned char* ws) {
    const int tid = threadIdx.x, wid = __builtin_amdgcn_readfirstlane(tid >> 6), lane = tid & 63;
    const unsigned ldsw = (unsigned)wid * 1024u;
    STAMP_DECL; STAMP();
    if (tid < 40) *(unsigned*)(ws + WS_FL + (size_t)(j * 40 + tid) * 64) = 0u;
    if (tid >= 256) { const int t = tid - 256; *(u32x4*)(ws + WS_UR + ((size_t)(j * 32 + (t >> 3)) * URP + 2048) * 2 + (t & 7) * 16) = (u32x4){0u, 0u, 0u, 0u}; }
    {
        const char* w = (const char*)((wid < 4) ? wg : wu) + (size_t)j * 14336 + (wid & 3) * 1024 + lane * 16;
        const char* xs = (const char*)x + (size_t)(64 * j + 8 * wid + (lane >> 3)) * 2048 + (lane & 7) * 16;
#pragma unroll 1
        for (int t = 2; t < 16; ++t) {
            const char* p = w + (size_t)t * 64 * 7168;
            GLDS16(xs + t * 128, ldsw); GLDS16(p, ldsw); GLDS16(p + 4096, ldsw); GLDS16(p + 8192, ldsw);
            if ((wid & 3) < 2) GLDS16(p + 12288, ldsw);
            WAIT_V(24);
        }
    }
    WAIT_V(0);
    __syncthreads();
    STAMP();
    const int cg = tid & 127, rg = tid >> 7;
    {
        const int s0 = 64 * j + 16 * rg;
        u32x4 kr[19], vr[16], wr4[4];
#pragma unroll
        for (int i = 0; i < 4; ++i) wr4[i] = ((const u32x4*)(shk + cg * 32))[i];
#pragma unroll
        for (int i = 0; i < 19; ++i) { const int s = s0 - 3 + i; kr[i] = *(const u32x4*)(kk + (size_t)(s < 0 ? 0 : s) * D_ + cg * 8); }
#pragma unroll
        for (int i = 0; i < 16; ++i) vr[i] = *(const u32x4*)(vv + (size_t)(s0 + i) * D_ + cg * 8);
        if (s0 == 0) kr[0] = kr[1] = kr[2] = (u32x4){0u, 0u, 0u, 0u};
#pragma unroll
        for (int cp = 0; cp < 4; ++cp) {
            const f32x2 w0 = {bf_lo(wr4[cp].x), bf_lo(wr4[cp].z)}, w1 = {bf_hi(wr4[cp].x), bf_hi(wr4[cp].z)};
            const f32x2 w2 = {bf_lo(wr4[cp].y), bf_lo(wr4[cp].w)}, w3 = {bf_hi(wr4[cp].y), bf_hi(wr4[cp].w)};
            f32x2 kf[19], o[16];
#pragma unroll
            for (int i = 0; i < 19; ++i) kf[i] = (f32x2){bf_lo(kr[i][cp]), bf_hi(kr[i][cp])};
#pragma unroll
            for (int i = 0; i < 16; ++i) {
                f32x2 a = kf[i] * w3; a = __builtin_elementwise_fma(kf[i + 1], w2, a); a = __builtin_elementwise_fma(kf[i + 2], w1, a); a = __builtin_elementwise_fma(kf[i + 3], w0, a);
                o[i] = a * (f32x2){bf_lo(vr[i][cp]), bf_hi(vr[i][cp])};
            }
#pragma unroll
            for (int hh = 0; hh < 2; ++hh)
#pragma unroll
                for (int e = 0; e < 2; ++e) {
                    u32x4 pk; pk.x = cvt_pk_bf16(o[8 * hh + 7][e], o[8 * hh + 6][e]); pk.y = cvt_pk_bf16(o[8 * hh + 5][e], o[8 * hh + 4][e]);
                    pk.z = cvt_pk_bf16(o[8 * hh + 3][e], o[8 * hh + 2][e]); pk.w = cvt_pk_bf16(o[8 * hh + 1][e], o[8 * hh][e]);
                    const int chunk = 7 - (2 * rg + hh);
                    *(LAS u32x4*)(lds + (cg * 8 + 2 * cp + e) * 128 + ((chunk ^ (cg & 7)) * 16)) = pk;
                }
        }
    }
    STAMP();
    {
        const u32x4 g0 = *(const u32x4*)(gw + lane * 8), g1 = *(const u32x4*)(gw + 512 + lane * 8);
        u32x4 a0[8], a1[8];
#pragma unroll
        for (int r = 0; r < 8; ++r) { const u16* xr = x + (size_t)(64 * j + wid * 8 + r) * D_ + lane * 8; a0[r] = *(const u32x4*)xr; a1[r] = *(const u32x4*)(xr + 512); }
        float acc[8];
#pragma unroll
        for (int r = 0; r < 8; ++r) { float t = 0.f;
#pragma unroll
            for (int i = 0; i < 4; ++i) {
                t += rbf(bf_lo(a0[r][i]) * bf_lo(g0[i])); t += rbf(bf_hi(a0[r][i]) * bf_hi(g0[i]));
                t += rbf(bf_lo(a1[r][i]) * bf_lo(g1[i])); t += rbf(bf_hi(a1[r][i]) * bf_hi(g1[i]));
            }
            acc[r] = t; }
#pragma unroll
        for (int o = 32; o >= 1; o >>= 1)
#pragma unroll
            for (int r = 0; r < 8; ++r) acc[r] += __shfl_xor(acc[r], o);
        float z = acc[0];
#pragma unroll
        for (int r = 1; r < 8; ++r) z = (lane == r) ? acc[r] : z;
        if (lane < 8) ((float*)(ws + WS_G))[64 * j + wid * 8 + lane] = rbf(1.0f / (1.0f + __expf(-rbf(z))));
    }
    STAMP();
    __syncthreads();
    STAMP();
    const int xb = (1984 - 64 * j) * 2;
    const auto urs = __builtin_amdgcn_make_buffer_rsrc((void*)(ws + WS_UR), 0, D_ * URP * 2, 0x00020000);
#pragma unroll 4
    for (int it = 0; it < 16; ++it) {
        const int ch = it * 64 + (tid >> 3), c = tid & 7;
        const u32x4 t = *(const LAS u32x4*)(lds + ch * 128 + ((c ^ ((ch >> 3) & 7)) * 16));
        __builtin_amdgcn_raw_buffer_store_b128(t, urs, ch * (URP * 2) + xb + c * 16, 0, HST_AUX);
    }
    STAMP();
    WAIT_V(0); STAMP(); STAMP_OUT(0);
}

__global__ __launch_bounds__(512, 2) void k1_gemm1_prep(const u16* __restrict__ x, const u16* __restrict__ kk, const u16* __restrict__ vv,
                                                        const u16* __restrict__ shk, const u16* __restrict__ wg, const u16* __restrict__ wu,
                                                        const u16* __restrict__ gw, const u16* __restrict__ ssmk, const u16* __restrict__ wd,
                                                        unsigned char* __restrict__ ws) {
    extern __shared__ __attribute__((aligned(16))) unsigned char smem[];
    LAS unsigned char* lds = (LAS unsigned char*)smem;
    const int xg = blockIdx.x & 7, l = blockIdx.x >> 3;
    if (l >= 28) { prep_block(lds, xg * 4 + (l - 28), x, kk, vv, shk, gw, wg, wu, ws); return; }
    STAMP_DECL; STAMP();
    const int pm = 4 * (xg & 1) + (l & 3), pn = 7 * (xg >> 1) + (l >> 2);
    const int tid = threadIdx.x, wid = __builtin_amdgcn_readfirstlane(tid >> 6), lane = tid & 63, wr = wid >> 2, wc = wid & 3, fr = lane & 15, fq = lane >> 4;
    constexpr int HTB = 16384;
    unsigned voffA[2], voffB[2];
#pragma unroll
    for (int i = 0; i < 2; ++i) {
        const int piece = wid + 8 * i;
        { const int row = piece * 8 + (lane >> 3), ch = (lane & 7) ^ (lane >> 3); voffA[i] = (unsigned)(row * 2048 + ch * 16); }
        { const int kr = piece * 4 + (lane >> 4), fs = (kr & 3) | (((kr >> 3) & 1) << 2), ch = (lane & 15) ^ (2 * fs); voffB[i] = (unsigned)(kr * (F_ * 2) + ch * 16); }
    }
    const unsigned ldsw = (unsigned)wid * 1024u;
    const int aoff0 = ((wr * 64 + fr) >> 3) * 1024 + (fr & 7) * 128 + ((fq ^ (fr & 7)) * 16), aoff1 = aoff0 ^ 64;
    const int tq = (lane & 15) >> 2, tp = lane & 3;
    const unsigned boff0 = (unsigned)(size_t)lds + 4 * HTB + (8 * fq + tq) * 256 + (((4 * wc + (tp >> 1)) ^ (2 * (tq | ((fq & 1) << 2)))) * 16) + 8 * (tp & 1), boff1 = boff0 ^ 32;
    const char* cA = (const char*)x + (size_t)pm * 256 * 2048;
    const char* cG = (const char*)wg + (size_t)pn * 256;
    const char* cU = (const char*)wu + (size_t)pn * 256;
    constexpr size_t kA = 128, kB = (size_t)64 * F_ * 2, hA = (size_t)128 * 2048;
#define SA(b, h) (((b) * 2 + (h)) * HTB)
#define SB(b, h) ((4 + (b) * 2 + (h)) * HTB)
#define STAGE(bufoff, gbase, voff) do { _Pragma("unroll") for (int _i = 0; _i < 2; ++_i) GLDS16((gbase) + (voff)[_i], (bufoff) + ldsw + _i * 8192); } while (0)
#define LDA(dst, b, h) do { _Pragma("unroll") for (int m = 0; m < 4; ++m) { dst[m][0] = LDS_B128(SA(b, h) + aoff0 + m * 2048); dst[m][1] = LDS_B128(SA(b, h) + aoff1 + m * 2048); } } while (0)
#define LDB(dst, b, h) do { \
        LDS_TR(dst[0], boff0, ((b) * 2 + (h)) * HTB); LDS_TR(dst[1], boff0, ((b) * 2 + (h)) * HTB + 1024); \
        LDS_TR(dst[2], boff0, ((b) * 2 + (h)) * HTB + 8192); LDS_TR(dst[3], boff0, ((b) * 2 + (h)) * HTB + 9216); \
        LDS_TR(dst[4], boff1, ((b) * 2 + (h)) * HTB); LDS_TR(dst[5], boff1, ((b) * 2 + (h)) * HTB + 1024); \
        LDS_TR(dst[6], boff1, ((b) * 2 + (h)) * HTB + 8192); LDS_TR(dst[7], boff1, ((b) * 2 + (h)) * HTB + 9216); } while (0)
#define WAIT_B(B) asm volatile("s_waitcnt lgkmcnt(0)" : "+v"(B[0]), "+v"(B[1]), "+v"(B[2]), "+v"(B[3]), "+v"(B[4]), "+v"(B[5]), "+v"(B[6]), "+v"(B[7]) :: "memory")
#define MMA(ai, bj, At, Bt) do { __builtin_amdgcn_s_setprio(1); _Pragma("unroll") for (int m = 0; m < 4; ++m) _Pragma("unroll") for (int n = 0; n < 2; ++n) _Pragma("unroll") for (int k = 0; k < 2; ++k) \
        acc[ai][bj][m][n] = __builtin_amdgcn_mfma_f32_16x16x32_bf16(CAT8(Bt[4 * n + 2 * k], Bt[4 * n + 2 * k + 1]), At[m][k], acc[ai][bj][m][n], 0, 0, 0); __builtin_amdgcn_s_setprio(0); } while (0)
    f32x4 acc[2][2][4][2];
#pragma unroll
    for (int a = 0; a < 2; ++a)
#pragma unroll
        for (int b = 0; b < 2; ++b)
#pragma unroll
            for (int m = 0; m < 4; ++m)
#pragma unroll
                for (int n = 0; n < 2; ++n) acc[a][b][m][n] = (f32x4){0.f, 0.f, 0.f, 0.f};
    bf16x8 At[4][2]; bf16x4 B0[8], B1[8];
    constexpr int nt = D_ / 64;
    STAGE(SB(0, 0), cG, voffB); STAGE(SA(0, 0), cA, voffA); STAGE(SB(0, 1), cU, voffB); STAGE(SA(0, 1), cA + hA, voffA);
    if (wr == 1) BAR();
    WAIT_V(4); BAR();
    STAGE(SB(1, 0), cG + kB, voffB); STAGE(SA(1, 0), cA + kA, voffA); STAGE(SB(1, 1), cU + kB, voffB);
    WAIT_V(6); BAR();
    STAMP();
    for (int t = 0; t < nt; t += 2) {
        const bool more = (t + 2 < nt);
        const char* a1 = cA + (size_t)(t + 1) * kA;
        const char* a2 = a1 + kA; const char* g2 = cG + (size_t)(t + 2) * kB; const char* u2 = cU + (size_t)(t + 2) * kB;
        const char* a3 = a2 + kA; const char* g3 = g2 + kB; const char* u3 = u2 + kB;
        LDB(B0, 0, 0); SCHED(); LDA(At, 0, 0); STAGE(SA(1, 1), a1 + hA, voffA);
        WAIT_L(8); BAR(); WAIT_B(B0); MMA(0, 0, At, B0); BAR(); SCHED();
        LDB(B1, 0, 1); if (more) STAGE(SB(0, 0), g2, voffB);
        BAR(); WAIT_B(B1); MMA(0, 1, At, B1); BAR();
        LDA(At, 0, 1); if (more) STAGE(SA(0, 0), a2, voffA);
        BAR(); WAIT_L(0); MMA(1, 0, At, B0); BAR(); SCHED();
        if (more) { STAGE(SB(0, 1), u2, voffB); WAIT_V(6); } else WAIT_V(0);
        BAR(); MMA(1, 1, At, B1); BAR();
        LDB(B0, 1, 0); SCHED(); LDA(At, 1, 0); if (more) STAGE(SA(0, 1), a2 + hA, voffA);
        WAIT_L(8); BAR(); WAIT_B(B0); MMA(0, 0, At, B0); BAR(); SCHED();
        LDB(B1, 1, 1); if (more) STAGE(SB(1, 0), g3, voffB);
        BAR(); WAIT_B(B1); MMA(0, 1, At, B1); BAR();
        LDA(At, 1, 1); if (more) STAGE(SA(1, 0), a3, voffA);
        BAR(); WAIT_L(0); MMA(1, 0, At, B0); BAR(); SCHED();
        if (more) { STAGE(SB(1, 1), u3, voffB); WAIT_V(6); }
        BAR(); MMA(1, 1, At, B1); BAR();
    }
    STAMP();
    {
        const int wgi = pm * 28 + pn;
        const char* pf = (const char*)wd + (size_t)wgi * 32768 + ldsw + lane * 16;
        const char* pf2 = (const char*)ssmk + (size_t)(wgi < 204 ? wgi : 204) * 20480 + (wid < 4 ? ldsw : 0u) + lane * 16;
        const auto hrs = __builtin_amdgcn_make_buffer_rsrc((void*)(ws + WS_H), 0, S_ * F_ * 2, 0x00020000);
        const int hp = ((pm * 16 + wr * 4) * 112 + pn * 4 + wc) * 1024 + lane * 16;
#pragma unroll
        for (int ai = 0; ai < 2; ++ai)
#pragma unroll
            for (int m = 0; m < 4; ++m) {
                const f32x4 g0 = acc[ai][0][m][0], g1 = acc[ai][0][m][1], u0 = acc[ai][1][m][0], u1 = acc[ai][1][m][1];
                u32x4 o;
                o.x = gelu_mul2(g0.xy, u0.xy); o.y = gelu_mul2(g0.zw, u0.zw); o.z = gelu_mul2(g1.xy, u1.xy); o.w = gelu_mul2(g1.zw, u1.zw);
                __builtin_amdgcn_raw_buffer_store_b128(o, hrs, hp, (ai * 8 + m) * 112 * 1024, HST_AUX);
                if (ai == 0) GLDS16(pf + m * 8192, 8 * HTB + ldsw);
                else if (m < 2) GLDS16(pf2 + ldsw * (wid >= 4) + m * 8192, 8 * HTB + ldsw);
                else if (m == 2 && wgi < 204) GLDS16(pf2 + 16384, 8 * HTB + ldsw);
            }
    }
    STAMP();
    WAIT_V(0);
    STAMP(); STAMP_OUT(0);
    if (wr == 0) BAR();
#undef SA
#undef SB
#undef STAGE
#undef LDA
#undef LDB
#undef MMA
#undef WAIT_B
}

constexpr int CV_UP = 4160, CV_HP = 4640, CV_HB = 4 * CV_UP, CV_CH = CV_HB + 2 * CV_HP;
constexpr int RB = 106496;
constexpr int NT2 = 28;
constexpr int CVF = 4;
constexpr int CV0 = CVF + 2;
constexpr int CVX = CV0 + 12, CVM = CVX + 1, CVS = CVM + 3, CVP = CVS + 2;
constexpr unsigned kSpinCap = 1u << 20;
constexpr int vm_b(int t) { return t + 2 < NT2 ? 2 : 0; }
constexpr int vm_a(int t) { return t + 3 < NT2 ? 2 : 0; }
constexpr int vm_f(int t) { return (t >= 0 && t < CVF) ? 3 : 0; }
constexpr int vm_s(int t) { return t == CVS ? 2 : 0; }
constexpr int vm_all(int t) { return vm_b(t) + vm_a(t) + vm_f(t) + vm_s(t); }
constexpr int vm_wait(int t) { return (t == CVF || t == CVS + 1) ? vm_all(t) : t + 2 < NT2 ? vm_a(t - 1) + vm_f(t - 1) + vm_s(t - 1) + vm_all(t) : 0; }
template <int N> __device__ __forceinline__ void wait_vm() { asm volatile("s_waitcnt vmcnt(%0)" :: "n"(N) : "memory"); }
__device__ constexpr int kSlA[3][12] = {{0,0,0,0,1,1,1,1,2,2,2,2}, {0,0,0,0,1,1,1,2,2,3,3,3}, {3,3,4,4,4,4,5,5,5,6,6,7}};
__device__ constexpr int kSlB[3][12] = {{0,1,2,3,0,1,2,3,0,1,2,3}, {4,5,6,7,4,5,6,4,5,4,3,2}, {0,1,0,1,2,3,0,1,2,0,1,0}};
constexpr int kUlo[3] = {0, 0, 3}, kUhi[3] = {2, 3, 7}, kHlo[3] = {0, 2, 0}, kHhi[3] = {3, 7, 3};

struct K2State {
    f32x4 acc[2][8];
    bf16x8 A[4][2];
    bf16x4 B[16];
    f32x4 cacc[8];
};

__global__ __launch_bounds__(512, 2) void k2_conv_gemm2(const u16* __restrict__ q, const u16* __restrict__ ssmk, const u16* __restrict__ ssmD,
                                                        const u16* __restrict__ wd, unsigned char* __restrict__ ws, u16* __restrict__ out) {
    extern __shared__ __attribute__((aligned(16))) unsigned char smem[];
    LAS unsigned char* lds = (LAS unsigned char*)smem;
    const unsigned lbase = (unsigned)(size_t)lds;
    const int tid = threadIdx.x, wid = __builtin_amdgcn_readfirstlane(tid >> 6), lane = tid & 63, fr = lane & 15, fq = lane >> 4;
    const int bid = blockIdx.x, xg = bid & 7, l = bid >> 3;
    const int z = xg & 1, tm = 8 * ((xg >> 1) & 1) + (l & 7), tn = 4 * (xg >> 2) + (l >> 3);
    const int kh = wid >> 2, wm = wid & 3;
    STAMP_DECL; STAMP();
    unsigned voffB[2];
#pragma unroll
    for (int i = 0; i < 2; ++i) { const int kr = 32 * kh + (wm + 4 * i) * 4 + (lane >> 4), ch = (lane & 15) ^ (2 * (kr & 7)); voffB[i] = (unsigned)(kr * 2048 + ch * 16); }
    const unsigned ldsw = (unsigned)(kh * 8192 + wm * 1024), voffA = (unsigned)lane * 16u;
    const char* cA0 = (const char*)(ws + WS_H) + ((size_t)(tm * 8 + wm * 2) * 112 + z * 56 + kh) * 1024;
    const char* cA1 = cA0 + 112 * 1024;
    const char* cB = (const char*)wd + (size_t)z * 1792 * 2048 + (size_t)tn * 256;
    constexpr size_t kB = (size_t)64 * 2048;
    K2State st; SEG_DECL;
#define STAGEB(t) do { const char* _b = cB + (size_t)(t) * kB; GLDS16(_b + voffB[0], RB + ((t) % 3) * 16384 + ldsw); GLDS16(_b + voffB[1], RB + ((t) % 3) * 16384 + ldsw + 4096); } while (0)
#define LOADA(t) do { asm volatile("global_load_dwordx4 %0, %1, %2" : "=v"(st.A[(t) & 3][0]) : "v"(voffA), "s"(cA0 + (size_t)(t) * 2048) : "memory"); \
        asm volatile("global_load_dwordx4 %0, %1, %2" : "=v"(st.A[(t) & 3][1]) : "v"(voffA), "s"(cA1 + (size_t)(t) * 2048) : "memory"); } while (0)
    const int ch = wid & 3, hf = wid >> 2, d = bid * 4 + ch, CB = ch * CV_CH;
    const unsigned dpair = ((const unsigned*)ssmD)[d >> 1];
    const float dval = (d & 1) ? bf_hi(dpair) : bf_lo(dpair);
    const u32x2 h03 = *(const u32x2*)(ssmk + (size_t)d * S_);
    STAGEB(0); LOADA(0); STAGEB(1); LOADA(1); LOADA(2);
    const char* ur = (const char*)(ws + WS_UR) + (size_t)d * URP * 2 + (3 - 2 * hf) * 2 + lane * 16;
    const auto hrs = __builtin_amdgcn_make_buffer_rsrc((void*)ssmk, 0, D_ * S_ * 2, 0x00020000);
    const int hvo = d * (S_ * 2) + (1 - hf) * 8 + lane * 16;
#define CFILL(i) do { if constexpr ((i) < 8) GLDS16(ur - ((i) >> 2) * 2 + ((i) & 3) * 1024, CB + (2 * hf + ((i) >> 2)) * CV_UP + ((i) & 3) * 1024); \
        else __builtin_amdgcn_raw_ptr_buffer_load_lds(hrs, (LAS void*)(lds + CB + CV_HB + hf * CV_HP + (29 - hf) * 16 + ((i) & 3) * 1024), 16, hvo + ((i) & 3) * 1024, 0, 0, 0); } while (0)
    if (lane < 28) *(LAS u32x4*)(lds + CB + CV_HB + hf * CV_HP + lane * 16) = (u32x4){0u, 0u, 0u, 0u};
    else if (lane < 34) { const int e = lane - 28; *(LAS u32x4*)(lds + CB + (2 * hf + e / 3) * CV_UP + 4096 + (e % 3) * 16) = (u32x4){0u, 0u, 0u, 0u}; }
    else if (lane == 34 && hf == 0) *(LAS u32x4*)(lds + CB + CV_HB + 28 * 16) = (u32x4){0u, 0u, h03.x, h03.y};
    const int tq = fr >> 2, tp4 = lane & 3;
    unsigned boff[8];
    { const unsigned b0 = lbase + RB + (32 * kh + 4 * fq + tq) * 256 + 8 * (tp4 & 1) + (((tp4 >> 1) ^ (2 * ((4 * fq + tq) & 7))) * 16);
#pragma unroll
      for (int n = 0; n < 8; ++n) boff[n] = b0 ^ (32 * n); }
    const unsigned ua = lbase + CB + (fr & 3) * CV_UP + 2 * (2040 - 8 * (fr >> 2) + 8 * fq) - 64 * 59 - 256 * hf;
    const unsigned ha = lbase + CB + CV_HB + (fr & 1) * CV_HP + 2 * (224 + 32 * (fr >> 1) + 8 * fq) - 192 - 256 * hf;
#pragma unroll
    for (int m = 0; m < 2; ++m)
#pragma unroll
        for (int n = 0; n < 8; ++n) st.acc[m][n] = (f32x4){0.f, 0.f, 0.f, 0.f};
#pragma unroll
    for (int n = 0; n < 8; ++n) st.cacc[n] = (f32x4){0.f, 0.f, 0.f, 0.f};
#define RDB(t, n) do { LDS_TR(st.B[2 * (n)], boff[n], ((t) % 3) * 16384); LDS_TR(st.B[2 * (n) + 1], boff[n], ((t) % 3) * 16384 + 4096); } while (0)
#define RD64(dst, vaddr, imm) asm volatile("ds_read_b64 %0, %1 offset:%2" : "=v"(dst) : "v"(vaddr), "i"(imm) : "memory")
    const int sl0 = 32 * (fr >> 1) + 4 * (fr & 1) + 8 * fq;
    const unsigned ypa = lbase + CB + CV_HB + (1 - hf) * CV_HP + lane * 16;
    const unsigned kva = lbase + CB + 3 * CV_UP + 2 * (2044 - 1024 * hf - sl0) - 1536;
    const int twa = 2 * hf * CV_CH + sl0 * 8 + (fr >> 1) * 8 + ch * 2;
    const unsigned tra = lbase + (tid >> 8) * CV_CH + (tid & 255) * 16 + ((tid & 255) >> 4) * 8;
#define RD128(dst, vaddr, imm) asm volatile("ds_read_b128 %0, %1 offset:%2" : "=v"(dst) : "v"(vaddr), "i"(imm) : "memory")
    gu32* flags = (gu32*)(ws + WS_FL);
    const auto yrs = __builtin_amdgcn_make_buffer_rsrc((void*)(ws + WS_Y2 + (size_t)bid * 16384), 0, 16384, 0x00020000);
    wait_vm<6>(); WAIT_L(0); BAR();
    if (kh) BAR();
    STAMP();

    auto trip = [&](auto tc) __attribute__((always_inline)) {
        constexpr int t = decltype(tc)::value;
        if constexpr (t == CV0 || t == CV0 + 6 || t == CVX || t == CVS + 1) STAMP();
        constexpr bool cv = (t >= CV0 && t < CVX);
        constexpr int cc = cv ? (t - CV0) / 3 : 0, sl = cv ? (t - CV0) % 3 : 0;
        constexpr int sgrp = cv ? 1 : (t >= CVX && t <= CVS) ? 2 : 0;
        SEG(sgrp, 0);
        bf16x8 U[8], Hf[8];
        f32x4 yp[2]; u32x2 kvp[4];
        constexpr bool mix = (t == CVM || t == CVM + 1);
        constexpr int mi = mix ? 2 * (t - CVM) : 0;
        if constexpr (mix) {
            RD128(yp[0], ypa, mi * 1024); RD128(yp[1], ypa, mi * 1024 + 1024);
            RD64(kvp[0], kva, 1536 - mi * 512); RD64(kvp[1], kva, 1024 - mi * 512);
        }
        if constexpr (t == CVS) { RD64(kvp[0], tra, 0); RD64(kvp[1], tra, 8); RD64(kvp[2], tra, 2 * CV_CH); RD64(kvp[3], tra, 2 * CV_CH + 8); }
        const char* bsrc = cB + (size_t)(t + 2) * kB;
        RDB(t, 0); RDB(t, 1);
        if constexpr (vm_b(t) != 0) GLDS16(bsrc + voffB[0], RB + ((t + 2) % 3) * 16384 + ldsw);
        SCHED(); RDB(t, 2); RDB(t, 3);
        if constexpr (vm_b(t) != 0) GLDS16(bsrc + voffB[1], RB + ((t + 2) % 3) * 16384 + ldsw + 4096);
        SCHED(); RDB(t, 4); RDB(t, 5);
        if constexpr (vm_a(t) != 0) asm volatile("global_load_dwordx4 %0, %1, %2" : "=v"(st.A[(t + 3) & 3][0]) : "v"(voffA), "s"(cA0 + (size_t)(t + 3) * 2048) : "memory");
        RDB(t, 6); RDB(t, 7);
        if constexpr (vm_a(t) != 0) asm volatile("global_load_dwordx4 %0, %1, %2" : "=v"(st.A[(t + 3) & 3][1]) : "v"(voffA), "s"(cA1 + (size_t)(t + 3) * 2048) : "memory");
        if constexpr (cv) {
#pragma unroll
            for (int a = kUlo[sl]; a <= kUhi[sl]; ++a) RD128(U[a], ua, 64 * (59 - 8 * a - cc));
#pragma unroll
            for (int b = kHlo[sl]; b <= kHhi[sl]; ++b) RD128(Hf[b], ha, 64 * (8 * b - cc + 3));
        }
        if constexpr (vm_f(t) != 0) { CFILL(3 * t); CFILL(3 * t + 1); CFILL(3 * t + 2); }
        if constexpr (t == CVX) {
            if (hf) {
#pragma unroll
                for (int i = 0; i < 4; ++i) { const f32x4 v = st.cacc[i]; st.cacc[i] = st.cacc[4 + i]; st.cacc[4 + i] = v; }
            }
#pragma unroll
            for (int i = 0; i < 4; ++i) *(LAS f32x4*)(lds + CB + CV_HB + hf * CV_HP + lane * 16 + i * 1024) = st.cacc[4 + i];
        }
        if constexpr (t == CVP) { if (tid == 256) __hip_atomic_store(flags + bid * 16, 1u, RLX_AGENT); }
        asm volatile("s_waitcnt lgkmcnt(0)" : "+v"(st.B[0]), "+v"(st.B[1]), "+v"(st.B[2]), "+v"(st.B[3]), "+v"(st.B[4]), "+v"(st.B[5]), "+v"(st.B[6]), "+v"(st.B[7]),
                     "+v"(st.B[8]), "+v"(st.B[9]), "+v"(st.B[10]), "+v"(st.B[11]), "+v"(st.B[12]), "+v"(st.B[13]), "+v"(st.B[14]), "+v"(st.B[15]),
                     "+v"(st.A[t & 3][0]), "+v"(st.A[t & 3][1]) :: "memory");
        if constexpr (cv && sl == 0) asm volatile("" : "+v"(U[0]), "+v"(U[1]), "+v"(U[2]), "+v"(Hf[0]), "+v"(Hf[1]), "+v"(Hf[2]), "+v"(Hf[3]) :: "memory");
        if constexpr (cv && sl == 1) asm volatile("" : "+v"(U[0]), "+v"(U[1]), "+v"(U[2]), "+v"(U[3]), "+v"(Hf[2]), "+v"(Hf[3]), "+v"(Hf[4]), "+v"(Hf[5]), "+v"(Hf[6]), "+v"(Hf[7]) :: "memory");
        if constexpr (cv && sl == 2) asm volatile("" : "+v"(U[3]), "+v"(U[4]), "+v"(U[5]), "+v"(U[6]), "+v"(U[7]), "+v"(Hf[0]), "+v"(Hf[1]), "+v"(Hf[2]), "+v"(Hf[3]) :: "memory");
        if constexpr (mix) asm volatile("" : "+v"(yp[0]), "+v"(yp[1]), "+v"(kvp[0]), "+v"(kvp[1]) :: "memory");
        if constexpr (t == CVS) asm volatile("" : "+v"(kvp[0]), "+v"(kvp[1]), "+v"(kvp[2]), "+v"(kvp[3]) :: "memory");
        SEG(sgrp, 1);
        SCHED(); BAR(); SCHED();
        SEG(sgrp, 2);
        __builtin_amdgcn_s_setprio(1);
#pragma unroll
        for (int n = 0; n < 8; ++n)
#pragma unroll
            for (int m = 0; m < 2; ++m)
                st.acc[m][n] = __builtin_amdgcn_mfma_f32_16x16x32_bf16(CAT8(st.B[2 * n], st.B[2 * n + 1]), st.A[t & 3][m], st.acc[m][n], 0, 0, 0);
        if constexpr (cv) {
#pragma unroll
            for (int i = 0; i < 12; ++i)
                st.cacc[kSlA[sl][i] + kSlB[sl][i]] = __builtin_amdgcn_mfma_f32_16x16x32_bf16(U[kSlA[sl][i]], Hf[kSlB[sl][i]], st.cacc[kSlA[sl][i] + kSlB[sl][i]], 0, 0, 0);
        }
        if constexpr (mix) {
#pragma unroll
            for (int i = 0; i < 2; ++i) {
                const f32x4 y = st.cacc[mi + i] + yp[i];
                const float k3 = bf_lo(kvp[i].x), k2 = bf_hi(kvp[i].x), k1 = bf_lo(kvp[i].y), k0 = bf_hi(kvp[i].y);
                const unsigned p01 = cvt_pk_bf16(fmaf(k0, dval, y[0]), fmaf(k1, dval, y[1]));
                const unsigned p23 = cvt_pk_bf16(fmaf(k2, dval, y[2]), fmaf(k3, dval, y[3]));
                LAS u16* tp = (LAS u16*)(lds + twa + (mi >> 1) * CV_CH + i * 2112);
                tp[0] = (u16)p01; tp[4] = (u16)(p01 >> 16); tp[8] = (u16)p23; tp[12] = (u16)(p23 >> 16);
            }
#pragma unroll
            for (int i = 0; i < 16; ++i) { __builtin_amdgcn_sched_group_barrier(0x8, 1, 0); __builtin_amdgcn_sched_group_barrier(0x2, 2, 0); }
        }
        if constexpr (t == CVS) {
            __builtin_amdgcn_raw_buffer_store_b128((u32x4){kvp[0].x, kvp[0].y, kvp[1].x, kvp[1].y}, yrs, tid * 16, 0, 16);
            __builtin_amdgcn_raw_buffer_store_b128((u32x4){kvp[2].x, kvp[2].y, kvp[3].x, kvp[3].y}, yrs, 8192 + tid * 16, 0, 16);
        }
        __builtin_amdgcn_s_setprio(0);
        SCHED();
        if constexpr (mix) WAIT_L(0);
        SEG(sgrp, 3);
        if constexpr (t + 1 < NT2) { wait_vm<vm_wait(t)>(); SEG(sgrp, 4); BAR(); SEG(sgrp, 5); } else { if (!kh) BAR(); }
        SCHED();
    };
#define TRIP4(t) trip(std::integral_constant<int, (t)>{}); trip(std::integral_constant<int, (t) + 1>{}); trip(std::integral_constant<int, (t) + 2>{}); trip(std::integral_constant<int, (t) + 3>{})
    TRIP4(0); TRIP4(4); TRIP4(8); TRIP4(12); TRIP4(16); TRIP4(20); TRIP4(24);
    STAMP();
    if (z != kh) {
#pragma unroll
        for (int n = 0; n < 8; ++n) { const f32x4 v = st.acc[0][n]; st.acc[0][n] = st.acc[1][n]; st.acc[1][n] = v; }
    }
#pragma unroll
    for (int n = 0; n < 8; ++n) *(LAS f32x4*)(lds + wid * 8192 + lane * 16 + n * 1024) = st.acc[1][n];
    if (kh) {
        WAIT_L(0); BAR();
        const auto xs = __builtin_amdgcn_make_buffer_rsrc((void*)(ws + WS_X + (size_t)bid * 32768), 0, 32768, 0x00020000);
#pragma unroll
        for (int n = 0; n < 8; ++n)
            __builtin_amdgcn_raw_buffer_store_b128(__builtin_bit_cast(u32x4, st.acc[0][n] + *(const LAS f32x4*)(lds + wm * 8192 + lane * 16 + n * 1024)), xs,
                                                   wm * 8192 + n * 1024 + lane * 16, 0, 16);
        STAMP();
        WAIT_V(0);
        if (lane == 0) __hip_atomic_store(flags + (256 + bid * 4 + wm) * 16, 1u, RLX_AGENT);
        STAMP(); STAMP_OUT(1); SEG_OUT();
    } else {
        const int s = tm * 128 + wm * 32 + z * 16 + fr, dcol = tn * 128 + 4 * fq;
        gu32* fp = flags + ((lane < 32) ? (tn * 32 + lane) : (256 + (bid ^ 1) * 4 + wm)) * 16;
        unsigned fv = __hip_atomic_load(fp, RLX_AGENT);
        u32x2 qv[8];
#pragma unroll
        for (int n = 0; n < 8; ++n) qv[n] = *(const u32x2*)(q + (size_t)s * D_ + dcol + 16 * n);
        const float g = ((const float*)(ws + WS_G))[s];
        const auto xp = __builtin_amdgcn_make_buffer_rsrc((void*)(ws + WS_X + (size_t)(bid ^ 1) * 32768), 0, 32768, 0x00020000);
        const auto y2 = __builtin_amdgcn_make_buffer_rsrc((void*)(ws + WS_Y2), 0, 256 * S_ * 8, 0x00020000);
        WAIT_L(0); BAR();
        unsigned spins = 0;
#pragma nounroll
        while (!__all(lane >= 32 || fv == 1u) && ++spins < kSpinCap) { asm volatile("s_sleep 1" ::: "memory"); fv = __hip_atomic_load(fp, RLX_AGENT); }
        asm volatile("" ::: "memory");
        u32x2 yv[8];
#pragma unroll
        for (int n = 0; n < 8; ++n) yv[n] = __builtin_amdgcn_raw_buffer_load_b64(y2, (((dcol >> 2) + 4 * n) * S_ + s) * 8, 0, 16);
#pragma unroll
        for (int n = 0; n < 8; ++n) st.acc[0][n] += *(const LAS f32x4*)(lds + (wm + 4) * 8192 + lane * 16 + n * 1024);
        f32x4 ga[8];
#pragma unroll
        for (int n = 0; n < 8; ++n) {
            ga[n][0] = rbf(g * rbf(bf_lo(qv[n].x) * bf_lo(yv[n].x))); ga[n][1] = rbf(g * rbf(bf_hi(qv[n].x) * bf_hi(yv[n].x)));
            ga[n][2] = rbf(g * rbf(bf_lo(qv[n].y) * bf_lo(yv[n].y))); ga[n][3] = rbf(g * rbf(bf_hi(qv[n].y) * bf_hi(yv[n].y)));
        }
        STAMP();
#pragma nounroll
        while (!__all(fv == 1u) && ++spins < kSpinCap) fv = __hip_atomic_load(fp, RLX_AGENT);
        asm volatile("" ::: "memory");
        STAMP();
        u32x4 pr[8];
#pragma unroll
        for (int n = 0; n < 8; ++n) pr[n] = __builtin_amdgcn_raw_buffer_load_b128(xp, wm * 8192 + n * 1024 + lane * 16, 0, 16);
        const float omg = rbf(1.0f - g);
#pragma unroll
        for (int n = 0; n < 8; ++n) {
            const f32x4 b = st.acc[0][n] + __builtin_bit_cast(f32x4, pr[n]);
            u32x2 o;
            o.x = cvt_pk_bf16(ga[n][0] + rbf(omg * rbf(b[0])), ga[n][1] + rbf(omg * rbf(b[1])));
            o.y = cvt_pk_bf16(ga[n][2] + rbf(omg * rbf(b[2])), ga[n][3] + rbf(omg * rbf(b[3])));
            *(u32x2*)(out + (size_t)s * D_ + dcol + 16 * n) = o;
        }
        STAMP(); WAIT_V(0); STAMP(); STAMP_OUT(1); SEG_OUT();
    }
#undef STAGEB
#undef LOADA
#undef RDB
#undef CFILL
#undef RD128
#undef RD64
#undef TRIP4
}

extern "C" void kernel_launch(void* const* d_in, const int* in_sizes, int n_in, void* d_out, int out_size, void* d_ws, size_t ws_size,
                              hipStream_t stream) {
    const u16* q = (const u16*)d_in[0];
    const u16* k = (const u16*)d_in[1];
    const u16* v = (const u16*)d_in[2];
    const u16* shk = (const u16*)d_in[3];
    const u16* ssmk = (const u16*)d_in[4];
    const u16* ssmD = (const u16*)d_in[5];
    const u16* wg = (const u16*)d_in[7];
    const u16* wu = (const u16*)d_in[8];
    const u16* wd = (const u16*)d_in[9];
    const u16* gw = (const u16*)d_in[10];
    (void)hipFuncSetAttribute((const void*)k1_gemm1_prep, hipFuncAttributeMaxDynamicSharedMemorySize, K1_LDS);
    (void)hipFuncSetAttribute((const void*)k2_conv_gemm2, hipFuncAttributeMaxDynamicSharedMemorySize, K2_LDS);
    k1_gemm1_prep<<<256, 512, K1_LDS, stream>>>(q, k, v, shk, wg, wu, gw, ssmk, wd, (unsigned char*)d_ws);
    k2_conv_gemm2<<<256, 512, K2_LDS, stream>>>(q, ssmk, ssmD, wd, (unsigned char*)d_ws, (u16*)d_out);
}
```

```cpp
#include <hip/hip_runtime.h>
#include <hip/hip_bf16.h>
#include <type_traits>

#define LAS __attribute__((address_space(3)))
#define GAS __attribute__((address_space(1)))
typedef unsigned short u16;
typedef short bf16x8 __attribute__((ext_vector_type(8)));
typedef short bf16x4 __attribute__((ext_vector_type(4)));
typedef float f32x4 __attribute__((ext_vector_type(4)));
typedef float f32x2 __attribute__((ext_vector_type(2)));
typedef unsigned u32x4 __attribute__((ext_vector_type(4)));
typedef unsigned u32x2 __attribute__((ext_vector_type(2)));
typedef GAS unsigned gu32;
typedef GAS unsigned long long gu64;
#define RLX_AGENT __ATOMIC_RELAXED, __HIP_MEMORY_SCOPE_AGENT

constexpr int S_ = 2048, D_ = 1024, F_ = 3584;
constexpr int URP = 2112;
constexpr size_t WS_H = 0;
constexpr size_t WS_UR = WS_H + (size_t)S_ * F_ * 2;
constexpr size_t WS_G = WS_UR + (size_t)D_ * URP * 2;
constexpr size_t WS_Y2 = WS_G + (size_t)S_ * 4;
constexpr size_t WS_X = WS_Y2 + (size_t)256 * S_ * 8;
constexpr size_t WS_FL = WS_X + (size_t)256 * 32768;
#define HST_AUX 16
constexpr int K1_LDS = 139264, K2_LDS = 163840;

__device__ __forceinline__ unsigned cvt_pk_bf16(float lo, float hi) {
    unsigned r; asm("v_cvt_pk_bf16_f32 %0, %1, %2" : "=v"(r) : "v"(lo), "v"(hi)); return r;
}
__device__ __forceinline__ float bf_lo(unsigned w) { return __uint_as_float(w << 16); }
__device__ __forceinline__ float bf_hi(unsigned w) { return __uint_as_float(w & 0xffff0000u); }
__device__ __forceinline__ float rbf(float x) { return bf_lo(cvt_pk_bf16(x, x)); }

#define GLDS16(gptr, ldsoff) __builtin_amdgcn_global_load_lds((const unsigned*)(gptr), (LAS unsigned*)(lds + (ldsoff)), 16, 0, 0)
#define LDS_B128(off) (*(const LAS bf16x8*)(lds + (off)))
#define LDS_TR(dst, vaddr, imm) asm volatile("ds_read_b64_tr_b16 %0, %1 offset:%2" : "=v"(dst) : "v"(vaddr), "i"(imm) : "memory")
#define CAT8(a, b) __builtin_shufflevector(a, b, 0, 1, 2, 3, 4, 5, 6, 7)
#define WAIT_V(n) asm volatile("s_waitcnt vmcnt(" #n ")" ::: "memory")
#define WAIT_L(n) asm volatile("s_waitcnt lgkmcnt(" #n ")" ::: "memory")
#define BAR() do { asm volatile("" ::: "memory"); __builtin_amdgcn_s_barrier(); asm volatile("" ::: "memory"); } while (0)
#define SCHED() __builtin_amdgcn_sched_barrier(0)

#define STAMP_DECL
#define STAMP()
#define STAMP_OUT(kidx)
#define SEG_DECL
#define SEG(grp, i)
#define SEG_OUT()
__device__ __forceinline__ unsigned gelu_mul2(f32x2 g, f32x2 u) {
    const f32x2 p = __builtin_elementwise_fma(g * g, (f32x2){-0.044715f * 2.3022082f, -0.044715f * 2.3022082f}, (f32x2){-2.3022082f, -2.3022082f});
    const f32x2 y = g * p;
    const f32x2 dd = (f32x2){__builtin_amdgcn_exp2f(fminf(y.x, 60.f)), __builtin_amdgcn_exp2f(fminf(y.y, 60.f))} + 1.0f;
    const float r = __builtin_amdgcn_rcpf(dd.x * dd.y);
    const f32x2 h = (g * u) * ((f32x2){dd.y, dd.x} * r);
    return cvt_pk_bf16(h.x, h.y);
}

__device__ __forceinline__ void prep_block(LAS unsigned char* lds, int j, const u16* x, const u16* kk, const u16* vv, const u16* shk, const u16* gw,
                                           const u16* wg, const u16* wu, unsigned char* ws) {
    const int tid = threadIdx.x, wid = __builtin_amdgcn_readfirstlane(tid >> 6), lane = tid & 63;
    const unsigned ldsw = (unsigned)wid * 1024u;
    STAMP_DECL; STAMP();
    if (tid < 40) *(unsigned*)(ws + WS_FL + (size_t)(j * 40 + tid) * 64) = 0u;
    if (tid >= 256) { const int t = tid - 256; *(u32x4*)(ws + WS_UR + ((size_t)(j * 32 + (t >> 3)) * URP + 2048) * 2 + (t & 7) * 16) = (u32x4){0u, 0u, 0u, 0u}; }
    {
        unsigned long long pc;
        asm volatile("s_getpc_b64 s[40:41]\n\ts_add_u32 s40, s40, _Z13k2_conv_gemm2PKtS0_S0_S0_PhPt@rel32@lo+4\n\ts_addc_u32 s41, s41, _Z13k2_conv_gemm2PKtS0_S0_S0_PhPt@rel32@hi+12\n\ts_mov_b64 %0, s[40:41]" : "=s"(pc) :: "s40", "s41", "scc");
        const char* cp = (const char*)pc + ldsw + lane * 16;
        GLDS16(cp, ldsw); GLDS16(cp + 8192, ldsw); if (wid < 3) GLDS16(cp + 16384, ldsw);
    }
    {
        const char* w = (const char*)((wid < 4) ? wg : wu) + (size_t)j * 14336 + (wid & 3) * 1024 + lane * 16;
        const char* xs = (const char*)x + (size_t)(64 * j + 8 * wid + (lane >> 3)) * 2048 + (lane & 7) * 16;
#pragma unroll 1
        for (int t = 2; t < 16; ++t) {
            const char* p = w + (size_t)t * 64 * 7168;
            GLDS16(xs + t * 128, ldsw); GLDS16(p, ldsw); GLDS16(p + 4096, ldsw); GLDS16(p + 8192, ldsw);
            if ((wid & 3) < 2) GLDS16(p + 12288, ldsw);
            WAIT_V(24);
        }
    }
    WAIT_V(0);
    __syncthreads();
    STAMP();
    const int cg = tid & 127, rg = tid >> 7;
    {
        const int s0 = 64 * j + 16 * rg;
        u32x4 kr[19], vr[16], wr4[4];
#pragma unroll
        for (int i = 0; i < 4; ++i) wr4[i] = ((const u32x4*)(shk + cg * 32))[i];
#pragma unroll
        for (int i = 0; i < 19; ++i) { const int s = s0 - 3 + i; kr[i] = *(const u32x4*)(kk + (size_t)(s < 0 ? 0 : s) * D_ + cg * 8); }
#pragma unroll
        for (int i = 0; i < 16; ++i) vr[i] = *(const u32x4*)(vv + (size_t)(s0 + i) * D_ + cg * 8);
        if (s0 == 0) kr[0] = kr[1] = kr[2] = (u32x4){0u, 0u, 0u, 0u};
#pragma unroll
        for (int cp = 0; cp < 4; ++cp) {
            const f32x2 w0 = {bf_lo(wr4[cp].x), bf_lo(wr4[cp].z)}, w1 = {bf_hi(wr4[cp].x), bf_hi(wr4[cp].z)};
            const f32x2 w2 = {bf_lo(wr4[cp].y), bf_lo(wr4[cp].w)}, w3 = {bf_hi(wr4[cp].y), bf_hi(wr4[cp].w)};
            f32x2 kf[19], o[16];
#pragma unroll
            for (int i = 0; i < 19; ++i) kf[i] = (f32x2){bf_lo(kr[i][cp]), bf_hi(kr[i][cp])};
#pragma unroll
            for (int i = 0; i < 16; ++i) {
                f32x2 a = kf[i] * w3; a = __builtin_elementwise_fma(kf[i + 1], w2, a); a = __builtin_elementwise_fma(kf[i + 2], w1, a); a = __builtin_elementwise_fma(kf[i + 3], w0, a);
                o[i] = a * (f32x2){bf_lo(vr[i][cp]), bf_hi(vr[i][cp])};
            }
#pragma unroll
            for (int hh = 0; hh < 2; ++hh)
#pragma unroll
                for (int e = 0; e < 2; ++e) {
                    u32x4 pk; pk.x = cvt_pk_bf16(o[8 * hh + 7][e], o[8 * hh + 6][e]); pk.y = cvt_pk_bf16(o[8 * hh + 5][e], o[8 * hh + 4][e]);
                    pk.z = cvt_pk_bf16(o[8 * hh + 3][e], o[8 * hh + 2][e]); pk.w = cvt_pk_bf16(o[8 * hh + 1][e], o[8 * hh][e]);
                    const int chunk = 7 - (2 * rg + hh);
                    *(LAS u32x4*)(lds + (cg * 8 + 2 * cp + e) * 128 + ((chunk ^ (cg & 7)) * 16)) = pk;
                }
        }
    }
    STAMP();
    {
        const u32x4 g0 = *(const u32x4*)(gw + lane * 8), g1 = *(const u32x4*)(gw + 512 + lane * 8);
        u32x4 a0[8], a1[8];
#pragma unroll
        for (int r = 0; r < 8; ++r) { const u16* xr = x + (size_t)(64 * j + wid * 8 + r) * D_ + lane * 8; a0[r] = *(const u32x4*)xr; a1[r] = *(const u32x4*)(xr + 512); }
        float acc[8];
#pragma unroll
        for (int r = 0; r < 8; ++r) { float t = 0.f;
#pragma unroll
            for (int i = 0; i < 4; ++i) {
                t += rbf(bf_lo(a0[r][i]) * bf_lo(g0[i])); t += rbf(bf_hi(a0[r][i]) * bf_hi(g0[i]));
                t += rbf(bf_lo(a1[r][i]) * bf_lo(g1[i])); t += rbf(bf_hi(a1[r][i]) * bf_hi(g1[i]));
            }
            acc[r] = t; }
#pragma unroll
        for (int o = 32; o >= 1; o >>= 1)
#pragma unroll
            for (int r = 0; r < 8; ++r) acc[r] += __shfl_xor(acc[r], o);
        float z = acc[0];
#pragma unroll
        for (int r = 1; r < 8; ++r) z = (lane == r) ? acc[r] : z;
        if (lane < 8) ((float*)(ws + WS_G))[64 * j + wid * 8 + lane] = rbf(1.0f / (1.0f + __expf(-rbf(z))));
    }
    STAMP();
    __syncthreads();
    STAMP();
    const int xb = (1984 - 64 * j) * 2;
    const auto urs = __builtin_amdgcn_make_buffer_rsrc((void*)(ws + WS_UR), 0, D_ * URP * 2, 0x00020000);
#pragma unroll 4
    for (int it = 0; it < 16; ++it) {
        const int ch = it * 64 + (tid >> 3), c = tid & 7;
        const u32x4 t = *(const LAS u32x4*)(lds + ch * 128 + ((c ^ ((ch >> 3) & 7)) * 16));
        __builtin_amdgcn_raw_buffer_store_b128(t, urs, ch * (URP * 2) + xb + c * 16, 0, HST_AUX);
    }
    STAMP();
    WAIT_V(0); STAMP(); STAMP_OUT(0);
}

__global__ __launch_bounds__(512, 2) void k1_gemm1_prep(const u16* __restrict__ x, const u16* __restrict__ kk, const u16* __restrict__ vv,
                                                        const u16* __restrict__ shk, const u16* __restrict__ wg, const u16* __restrict__ wu,
                                                        const u16* __restrict__ gw, const u16* __restrict__ ssmk, const u16* __restrict__ wd,
                                                        unsigned char* __restrict__ ws) {
    extern __shared__ __attribute__((aligned(16))) unsigned char smem[];
    LAS unsigned char* lds = (LAS unsigned char*)smem;
    const int xg = blockIdx.x & 7, l = blockIdx.x >> 3;
    if (l >= 28) { prep_block(lds, xg * 4 + (l - 28), x, kk, vv, shk, gw, wg, wu, ws); return; }
    STAMP_DECL; STAMP();
    const int pm = 4 * (xg & 1) + (l & 3), pn = 7 * (xg >> 1) + (l >> 2);
    const int tid = threadIdx.x, wid = __builtin_amdgcn_readfirstlane(tid >> 6), lane = tid & 63, wr = wid >> 2, wc = wid & 3, fr = lane & 15, fq = lane >> 4;
    constexpr int HTB = 16384;
    unsigned voffA[2], voffB[2];
#pragma unroll
    for (int i = 0; i < 2; ++i) {
        const int piece = wid + 8 * i;
        { const int row = piece * 8 + (lane >> 3), ch = (lane & 7) ^ (lane >> 3); voffA[i] = (unsigned)(row * 2048 + ch * 16); }
        { const int kr = piece * 4 + (lane >> 4), fs = (kr & 3) | (((kr >> 3) & 1) << 2), ch = (lane & 15) ^ (2 * fs); voffB[i] = (unsigned)(kr * (F_ * 2) + ch * 16); }
    }
    const unsigned ldsw = (unsigned)wid * 1024u;
    const int aoff0 = ((wr * 64 + fr) >> 3) * 1024 + (fr & 7) * 128 + ((fq ^ (fr & 7)) * 16), aoff1 = aoff0 ^ 64;
    const int tq = (lane & 15) >> 2, tp = lane & 3;
    const unsigned boff0 = (unsigned)(size_t)lds + 4 * HTB + (8 * fq + tq) * 256 + (((4 * wc + (tp >> 1)) ^ (2 * (tq | ((fq & 1) << 2)))) * 16) + 8 * (tp & 1), boff1 = boff0 ^ 32;
    const char* cA = (const char*)x + (size_t)pm * 256 * 2048;
    const char* cG = (const char*)wg + (size_t)pn * 256;
    const char* cU = (const char*)wu + (size_t)pn * 256;
    constexpr size_t kA = 128, kB = (size_t)64 * F_ * 2, hA = (size_t)128 * 2048;
#define SA(b, h) (((b) * 2 + (h)) * HTB)
#define SB(b, h) ((4 + (b) * 2 + (h)) * HTB)
#define STAGE(bufoff, gbase, voff) do { _Pragma("unroll") for (int _i = 0; _i < 2; ++_i) GLDS16((gbase) + (voff)[_i], (bufoff) + ldsw + _i * 8192); } while (0)
#define LDA(dst, b, h) do { _Pragma("unroll") for (int m = 0; m < 4; ++m) { dst[m][0] = LDS_B128(SA(b, h) + aoff0 + m * 2048); dst[m][1] = LDS_B128(SA(b, h) + aoff1 + m * 2048); } } while (0)
#define LDB(dst, b, h) do { \
        LDS_TR(dst[0], boff0, ((b) * 2 + (h)) * HTB); LDS_TR(dst[1], boff0, ((b) * 2 + (h)) * HTB + 1024); \
        LDS_TR(dst[2], boff0, ((b) * 2 + (h)) * HTB + 8192); LDS_TR(dst[3], boff0, ((b) * 2 + (h)) * HTB + 9216); \
        LDS_TR(dst[4], boff1, ((b) * 2 + (h)) * HTB); LDS_TR(dst[5], boff1, ((b) * 2 + (h)) * HTB + 1024); \
        LDS_TR(dst[6], boff1, ((b) * 2 + (h)) * HTB + 8192); LDS_TR(dst[7], boff1, ((b) * 2 + (h)) * HTB + 9216); } while (0)
#define WAIT_B(B) asm volatile("s_waitcnt lgkmcnt(0)" : "+v"(B[0]), "+v"(B[1]), "+v"(B[2]), "+v"(B[3]), "+v"(B[4]), "+v"(B[5]), "+v"(B[6]), "+v"(B[7]) :: "memory")
#define MMA(ai, bj, At, Bt) do { __builtin_amdgcn_s_setprio(1); _Pragma("unroll") for (int m = 0; m < 4; ++m) _Pragma("unroll") for (int n = 0; n < 2; ++n) _Pragma("unroll") for (int k = 0; k < 2; ++k) \
        acc[ai][bj][m][n] = __builtin_amdgcn_mfma_f32_16x16x32_bf16(CAT8(Bt[4 * n + 2 * k], Bt[4 * n + 2 * k + 1]), At[m][k], acc[ai][bj][m][n], 0, 0, 0); __builtin_amdgcn_s_setprio(0); } while (0)
    f32x4 acc[2][2][4][2];
#pragma unroll
    for (int a = 0; a < 2; ++a)
#pragma unroll
        for (int b = 0; b < 2; ++b)
#pragma unroll
            for (int m = 0; m < 4; ++m)
#pragma unroll
                for (int n = 0; n < 2; ++n) acc[a][b][m][n] = (f32x4){0.f, 0.f, 0.f, 0.f};
    bf16x8 At[4][2]; bf16x4 B0[8], B1[8];
    constexpr int nt = D_ / 64;
    STAGE(SB(0, 0), cG, voffB); STAGE(SA(0, 0), cA, voffA); STAGE(SB(0, 1), cU, voffB); STAGE(SA(0, 1), cA + hA, voffA);
    if (wr == 1) BAR();
    WAIT_V(4); BAR();
    STAGE(SB(1, 0), cG + kB, voffB); STAGE(SA(1, 0), cA + kA, voffA); STAGE(SB(1, 1), cU + kB, voffB);
    WAIT_V(6); BAR();
    STAMP();
    for (int t = 0; t < nt; t += 2) {
        const bool more = (t + 2 < nt);
        const char* a1 = cA + (size_t)(t + 1) * kA;
        const char* a2 = a1 + kA; const char* g2 = cG + (size_t)(t + 2) * kB; const char* u2 = cU + (size_t)(t + 2) * kB;
        const char* a3 = a2 + kA; const char* g3 = g2 + kB; const char* u3 = u2 + kB;
        LDB(B0, 0, 0); SCHED(); LDA(At, 0, 0); STAGE(SA(1, 1), a1 + hA, voffA);
        WAIT_L(8); BAR(); WAIT_B(B0); MMA(0, 0, At, B0); BAR(); SCHED();
        LDB(B1, 0, 1); if (more) STAGE(SB(0, 0), g2, voffB);
        BAR(); WAIT_B(B1); MMA(0, 1, At, B1); BAR();
        LDA(At, 0, 1); if (more) STAGE(SA(0, 0), a2, voffA);
        BAR(); WAIT_L(0); MMA(1, 0, At, B0); BAR(); SCHED();
        if (more) { STAGE(SB(0, 1), u2, voffB); WAIT_V(6); } else WAIT_V(0);
        BAR(); MMA(1, 1, At, B1); BAR();
        LDB(B0, 1, 0); SCHED(); LDA(At, 1, 0); if (more) STAGE(SA(0, 1), a2 + hA, voffA);
        WAIT_L(8); BAR(); WAIT_B(B0); MMA(0, 0, At, B0); BAR(); SCHED();
        LDB(B1, 1, 1); if (more) STAGE(SB(1, 0), g3, voffB);
        BAR(); WAIT_B(B1); MMA(0, 1, At, B1); BAR();
        LDA(At, 1, 1); if (more) STAGE(SA(1, 0), a3, voffA);
        BAR(); WAIT_L(0); MMA(1, 0, At, B0); BAR(); SCHED();
        if (more) { STAGE(SB(1, 1), u3, voffB); WAIT_V(6); }
        BAR(); MMA(1, 1, At, B1); BAR();
    }
    STAMP();
    {
        const int wgi = pm * 28 + pn;
        const char* pf = (const char*)wd + (size_t)wgi * 32768 + ldsw + lane * 16;
        const char* pf2 = (const char*)ssmk + (size_t)(wgi < 204 ? wgi : 204) * 20480 + (wid < 4 ? ldsw : 0u) + lane * 16;
        const auto hrs = __builtin_amdgcn_make_buffer_rsrc((void*)(ws + WS_H), 0, S_ * F_ * 2, 0x00020000);
        const int hp = ((pm * 16 + wr * 4) * 112 + pn * 4 + wc) * 1024 + lane * 16;
#pragma unroll
        for (int ai = 0; ai < 2; ++ai)
#pragma unroll
            for (int m = 0; m < 4; ++m) {
                const f32x4 g0 = acc[ai][0][m][0], g1 = acc[ai][0][m][1], u0 = acc[ai][1][m][0], u1 = acc[ai][1][m][1];
                u32x4 o;
                o.x = gelu_mul2(g0.xy, u0.xy); o.y = gelu_mul2(g0.zw, u0.zw); o.z = gelu_mul2(g1.xy, u1.xy); o.w = gelu_mul2(g1.zw, u1.zw);
                __builtin_amdgcn_raw_buffer_store_b128(o, hrs, hp, (ai * 8 + m) * 112 * 1024, HST_AUX);
                if (ai == 0) GLDS16(pf + m * 8192, 8 * HTB + ldsw);
                else if (m < 2) GLDS16(pf2 + ldsw * (wid >= 4) + m * 8192, 8 * HTB + ldsw);
                else if (m == 2 && wgi < 204) GLDS16(pf2 + 16384, 8 * HTB + ldsw);
            }
    }
    STAMP();
    WAIT_V(0);
    STAMP(); STAMP_OUT(0);
    if (wr == 0) BAR();
#undef SA
#undef SB
#undef STAGE
#undef LDA
#undef LDB
#undef MMA
#undef WAIT_B
}

constexpr int CV_UP = 4160, CV_HP = 4640, CV_HB = 4 * CV_UP, CV_CH = CV_HB + 2 * CV_HP;
constexpr int RB = 106496;
constexpr int NT2 = 28;
constexpr int CVF = 4;
constexpr int CV0 = CVF + 2;
constexpr int CVX = CV0 + 12, CVM = CVX + 1, CVS = CVM + 3, CVP = CVS + 2;
constexpr unsigned kSpinCap = 1u << 20;
constexpr int vm_b(int t) { return t + 2 < NT2 ? 2 : 0; }
constexpr int vm_a(int t) { return t + 3 < NT2 ? 2 : 0; }
constexpr int vm_f(int t) { return (t >= 0 && t < CVF) ? 3 : 0; }
constexpr int vm_s(int t) { return t == CVS ? 2 : 0; }
constexpr int vm_all(int t) { return vm_b(t) + vm_a(t) + vm_f(t) + vm_s(t); }
constexpr int vm_wait(int t) { return (t == CVF || t == CVS + 1) ? vm_all(t) : t + 2 < NT2 ? vm_a(t - 1) + vm_f(t - 1) + vm_s(t - 1) + vm_all(t) : 0; }
template <int N> __device__ __forceinline__ void wait_vm() { asm volatile("s_waitcnt vmcnt(%0)" :: "n"(N) : "memory"); }
__device__ constexpr int kSlA[3][12] = {{0,0,0,0,1,1,1,1,2,2,2,2}, {0,0,0,0,1,1,1,2,2,3,3,3}, {3,3,4,4,4,4,5,5,5,6,6,7}};
__device__ constexpr int kSlB[3][12] = {{0,1,2,3,0,1,2,3,0,1,2,3}, {4,5,6,7,4,5,6,4,5,4,3,2}, {0,1,0,1,2,3,0,1,2,0,1,0}};
constexpr int kUlo[3] = {0, 0, 3}, kUhi[3] = {2, 3, 7}, kHlo[3] = {0, 2, 0}, kHhi[3] = {3, 7, 3};

struct K2State {
    f32x4 acc[2][8];
    bf16x8 A[4][2];
    bf16x4 B[16];
    f32x4 cacc[8];
};

__global__ __launch_bounds__(512, 2) void k2_conv_gemm2(const u16* __restrict__ q, const u16* __restrict__ ssmk, const u16* __restrict__ ssmD,
                                                        const u16* __restrict__ wd, unsigned char* __restrict__ ws, u16* __restrict__ out) {
    extern __shared__ __attribute__((aligned(16))) unsigned char smem[];
    LAS unsigned char* lds = (LAS unsigned char*)smem;
    const unsigned lbase = (unsigned)(size_t)lds;
    const int tid = threadIdx.x, wid = __builtin_amdgcn_readfirstlane(tid >> 6), lane = tid & 63, fr = lane & 15, fq = lane >> 4;
    const int bid = blockIdx.x, xg = bid & 7, l = bid >> 3;
    const int z = xg & 1, tm = 8 * ((xg >> 1) & 1) + (l & 7), tn = 4 * (xg >> 2) + (l >> 3);
    const int kh = wid >> 2, wm = wid & 3;
    STAMP_DECL; STAMP();
    unsigned voffB[2];
#pragma unroll
    for (int i = 0; i < 2; ++i) { const int kr = 32 * kh + (wm + 4 * i) * 4 + (lane >> 4), ch = (lane & 15) ^ (2 * (kr & 7)); voffB[i] = (unsigned)(kr * 2048 + ch * 16); }
    const unsigned ldsw = (unsigned)(kh * 8192 + wm * 1024), voffA = (unsigned)lane * 16u;
    const char* cA0 = (const char*)(ws + WS_H) + ((size_t)(tm * 8 + wm * 2) * 112 + z * 56 + kh) * 1024;
    const char* cA1 = cA0 + 112 * 1024;
    const char* cB = (const char*)wd + (size_t)z * 1792 * 2048 + (size_t)tn * 256;
    constexpr size_t kB = (size_t)64 * 2048;
    K2State st; SEG_DECL;
#define STAGEB(t) do { const char* _b = cB + (size_t)(t) * kB; GLDS16(_b + voffB[0], RB + ((t) % 3) * 16384 + ldsw); GLDS16(_b + voffB[1], RB + ((t) % 3) * 16384 + ldsw + 4096); } while (0)
#define LOADA(t) do { asm volatile("global_load_dwordx4 %0, %1, %2" : "=v"(st.A[(t) & 3][0]) : "v"(voffA), "s"(cA0 + (size_t)(t) * 2048) : "memory"); \
        asm volatile("global_load_dwordx4 %0, %1, %2" : "=v"(st.A[(t) & 3][1]) : "v"(voffA), "s"(cA1 + (size_t)(t) * 2048) : "memory"); } while (0)
    const int ch = wid & 3, hf = wid >> 2, d = bid * 4 + ch, CB = ch * CV_CH;
    const unsigned dpair = ((const unsigned*)ssmD)[d >> 1];
    const float dval = (d & 1) ? bf_hi(dpair) : bf_lo(dpair);
    const u32x2 h03 = *(const u32x2*)(ssmk + (size_t)d * S_);
    STAGEB(0); LOADA(0); STAGEB(1); LOADA(1); LOADA(2);
    const char* ur = (const char*)(ws + WS_UR) + (size_t)d * URP * 2 + (3 - 2 * hf) * 2 + lane * 16;
    const auto hrs = __builtin_amdgcn_make_buffer_rsrc((void*)ssmk, 0, D_ * S_ * 2, 0x00020000);
    const int hvo = d * (S_ * 2) + (1 - hf) * 8 + lane * 16;
#define CFILL(i) do { if constexpr ((i) < 8) GLDS16(ur - ((i) >> 2) * 2 + ((i) & 3) * 1024, CB + (2 * hf + ((i) >> 2)) * CV_UP + ((i) & 3) * 1024); \
        else __builtin_amdgcn_raw_ptr_buffer_load_lds(hrs, (LAS void*)(lds + CB + CV_HB + hf * CV_HP + (29 - hf) * 16 + ((i) & 3) * 1024), 16, hvo + ((i) & 3) * 1024, 0, 0, 0); } while (0)
    if (lane < 28) *(LAS u32x4*)(lds + CB + CV_HB + hf * CV_HP + lane * 16) = (u32x4){0u, 0u, 0u, 0u};
    else if (lane < 34) { const int e = lane - 28; *(LAS u32x4*)(lds + CB + (2 * hf + e / 3) * CV_UP + 4096 + (e % 3) * 16) = (u32x4){0u, 0u, 0u, 0u}; }
    else if (lane == 34 && hf == 0) *(LAS u32x4*)(lds + CB + CV_HB + 28 * 16) = (u32x4){0u, 0u, h03.x, h03.y};
    const int tq = fr >> 2, tp4 = lane & 3;
    unsigned boff[8];
    { const unsigned b0 = lbase + RB + (32 * kh + 4 * fq + tq) * 256 + 8 * (tp4 & 1) + (((tp4 >> 1) ^ (2 * ((4 * fq + tq) & 7))) * 16);
#pragma unroll
      for (int n = 0; n < 8; ++n) boff[n] = b0 ^ (32 * n); }
    const unsigned ua = lbase + CB + (fr & 3) * CV_UP + 2 * (2040 - 8 * (fr >> 2) + 8 * fq) - 64 * 59 - 256 * hf;
    const unsigned ha = lbase + CB + CV_HB + (fr & 1) * CV_HP + 2 * (224 + 32 * (fr >> 1) + 8 * fq) - 192 - 256 * hf;
#pragma unroll
    for (int m = 0; m < 2; ++m)
#pragma unroll
        for (int n = 0; n < 8; ++n) st.acc[m][n] = (f32x4){0.f, 0.f, 0.f, 0.f};
#pragma unroll
    for (int n = 0; n < 8; ++n) st.cacc[n] = (f32x4){0.f, 0.f, 0.f, 0.f};
#define RDB(t, n) do { LDS_TR(st.B[2 * (n)], boff[n], ((t) % 3) * 16384); LDS_TR(st.B[2 * (n) + 1], boff[n], ((t) % 3) * 16384 + 4096); } while (0)
#define RD64(dst, vaddr, imm) asm volatile("ds_read_b64 %0, %1 offset:%2" : "=v"(dst) : "v"(vaddr), "i"(imm) : "memory")
    const int sl0 = 32 * (fr >> 1) + 4 * (fr & 1) + 8 * fq;
    const unsigned ypa = lbase + CB + CV_HB + (1 - hf) * CV_HP + lane * 16;
    const unsigned kva = lbase + CB + 3 * CV_UP + 2 * (2044 - 1024 * hf - sl0) - 1536;
    const int twa = 2 * hf * CV_CH + sl0 * 8 + (fr >> 1) * 8 + ch * 2;
    const unsigned tra = lbase + (tid >> 8) * CV_CH + (tid & 255) * 16 + ((tid & 255) >> 4) * 8;
#define RD128(dst, vaddr, imm) asm volatile("ds_read_b128 %0, %1 offset:%2" : "=v"(dst) : "v"(vaddr), "i"(imm) : "memory")
    gu32* flags = (gu32*)(ws + WS_FL);
    const auto yrs = __builtin_amdgcn_make_buffer_rsrc((void*)(ws + WS_Y2 + (size_t)bid * 16384), 0, 16384, 0x00020000);
    wait_vm<6>(); WAIT_L(0); BAR();
    if (kh) BAR();
    STAMP();

    auto trip = [&](auto tc) __attribute__((always_inline)) {
        constexpr int t = decltype(tc)::value;
        if constexpr (t == CV0 || t == CV0 + 6 || t == CVX || t == CVS + 1) STAMP();
        constexpr bool cv = (t >= CV0 && t < CVX);
        constexpr int cc = cv ? (t - CV0) / 3 : 0, sl = cv ? (t - CV0) % 3 : 0;
        constexpr int sgrp = cv ? 1 : (t >= CVX && t <= CVS) ? 2 : 0;
        SEG(sgrp, 0);
        bf16x8 U[8], Hf[8];
        f32x4 yp[2]; u32x2 kvp[4];
        constexpr bool mix = (t == CVM || t == CVM + 1);
        constexpr int mi = mix ? 2 * (t - CVM) : 0;
        if constexpr (mix) {
            RD128(yp[0], ypa, mi * 1024); RD128(yp[1], ypa, mi * 1024 + 1024);
            RD64(kvp[0], kva, 1536 - mi * 512); RD64(kvp[1], kva, 1024 - mi * 512);
        }
        if constexpr (t == CVS) { RD64(kvp[0], tra, 0); RD64(kvp[1], tra, 8); RD64(kvp[2], tra, 2 * CV_CH); RD64(kvp[3], tra, 2 * CV_CH + 8); }
        const char* bsrc = cB + (size_t)(t + 2) * kB;
        RDB(t, 0); RDB(t, 1);
        if constexpr (vm_b(t) != 0) GLDS16(bsrc + voffB[0], RB + ((t + 2) % 3) * 16384 + ldsw);
        SCHED(); RDB(t, 2); RDB(t, 3);
        if constexpr (vm_b(t) != 0) GLDS16(bsrc + voffB[1], RB + ((t + 2) % 3) * 16384 + ldsw + 4096);
        SCHED(); RDB(t, 4); RDB(t, 5);
        if constexpr (vm_a(t) != 0) asm volatile("global_load_dwordx4 %0, %1, %2" : "=v"(st.A[(t + 3) & 3][0]) : "v"(voffA), "s"(cA0 + (size_t)(t + 3) * 2048) : "memory");
        RDB(t, 6); RDB(t, 7);
        if constexpr (vm_a(t) != 0) asm volatile("global_load_dwordx4 %0, %1, %2" : "=v"(st.A[(t + 3) & 3][1]) : "v"(voffA), "s"(cA1 + (size_t)(t + 3) * 2048) : "memory");
        if constexpr (cv) {
#pragma unroll
            for (int a = kUlo[sl]; a <= kUhi[sl]; ++a) RD128(U[a], ua, 64 * (59 - 8 * a - cc));
#pragma unroll
            for (int b = kHlo[sl]; b <= kHhi[sl]; ++b) RD128(Hf[b], ha, 64 * (8 * b - cc + 3));
        }
        if constexpr (vm_f(t) != 0) { CFILL(3 * t); CFILL(3 * t + 1); CFILL(3 * t + 2); }
        if constexpr (t == CVX) {
            if (hf) {
#pragma unroll
                for (int i = 0; i < 4; ++i) { const f32x4 v = st.cacc[i]; st.cacc[i] = st.cacc[4 + i]; st.cacc[4 + i] = v; }
            }
#pragma unroll
            for (int i = 0; i < 4; ++i) *(LAS f32x4*)(lds + CB + CV_HB + hf * CV_HP + lane * 16 + i * 1024) = st.cacc[4 + i];
        }
        if constexpr (t == CVP) { if (tid == 256) __hip_atomic_store(flags + bid * 16, 1u, RLX_AGENT); }
        asm volatile("s_waitcnt lgkmcnt(0)" : "+v"(st.B[0]), "+v"(st.B[1]), "+v"(st.B[2]), "+v"(st.B[3]), "+v"(st.B[4]), "+v"(st.B[5]), "+v"(st.B[6]), "+v"(st.B[7]),
                     "+v"(st.B[8]), "+v"(st.B[9]), "+v"(st.B[10]), "+v"(st.B[11]), "+v"(st.B[12]), "+v"(st.B[13]), "+v"(st.B[14]), "+v"(st.B[15]),
                     "+v"(st.A[t & 3][0]), "+v"(st.A[t & 3][1]) :: "memory");
        if constexpr (cv && sl == 0) asm volatile("" : "+v"(U[0]), "+v"(U[1]), "+v"(U[2]), "+v"(Hf[0]), "+v"(Hf[1]), "+v"(Hf[2]), "+v"(Hf[3]) :: "memory");
        if constexpr (cv && sl == 1) asm volatile("" : "+v"(U[0]), "+v"(U[1]), "+v"(U[2]), "+v"(U[3]), "+v"(Hf[2]), "+v"(Hf[3]), "+v"(Hf[4]), "+v"(Hf[5]), "+v"(Hf[6]), "+v"(Hf[7]) :: "memory");
        if constexpr (cv && sl == 2) asm volatile("" : "+v"(U[3]), "+v"(U[4]), "+v"(U[5]), "+v"(U[6]), "+v"(U[7]), "+v"(Hf[0]), "+v"(Hf[1]), "+v"(Hf[2]), "+v"(Hf[3]) :: "memory");
        if constexpr (mix) asm volatile("" : "+v"(yp[0]), "+v"(yp[1]), "+v"(kvp[0]), "+v"(kvp[1]) :: "memory");
        if constexpr (t == CVS) asm volatile("" : "+v"(kvp[0]), "+v"(kvp[1]), "+v"(kvp[2]), "+v"(kvp[3]) :: "memory");
        SEG(sgrp, 1);
        SCHED(); BAR(); SCHED();
        SEG(sgrp, 2);
        __builtin_amdgcn_s_setprio(1);
#pragma unroll
        for (int n = 0; n < 8; ++n)
#pragma unroll
            for (int m = 0; m < 2; ++m)
                st.acc[m][n] = __builtin_amdgcn_mfma_f32_16x16x32_bf16(CAT8(st.B[2 * n], st.B[2 * n + 1]), st.A[t & 3][m], st.acc[m][n], 0, 0, 0);
        if constexpr (cv) {
#pragma unroll
            for (int i = 0; i < 12; ++i)
                st.cacc[kSlA[sl][i] + kSlB[sl][i]] = __builtin_amdgcn_mfma_f32_16x16x32_bf16(U[kSlA[sl][i]], Hf[kSlB[sl][i]], st.cacc[kSlA[sl][i] + kSlB[sl][i]], 0, 0, 0);
        }
        if constexpr (mix) {
#pragma unroll
            for (int i = 0; i < 2; ++i) {
                const f32x4 y = st.cacc[mi + i] + yp[i];
                const float k3 = bf_lo(kvp[i].x), k2 = bf_hi(kvp[i].x), k1 = bf_lo(kvp[i].y), k0 = bf_hi(kvp[i].y);
                const unsigned p01 = cvt_pk_bf16(fmaf(k0, dval, y[0]), fmaf(k1, dval, y[1]));
                const unsigned p23 = cvt_pk_bf16(fmaf(k2, dval, y[2]), fmaf(k3, dval, y[3]));
                LAS u16* tp = (LAS u16*)(lds + twa + (mi >> 1) * CV_CH + i * 2112);
                tp[0] = (u16)p01; tp[4] = (u16)(p01 >> 16); tp[8] = (u16)p23; tp[12] = (u16)(p23 >> 16);
            }
#pragma unroll
            for (int i = 0; i < 16; ++i) { __builtin_amdgcn_sched_group_barrier(0x8, 1, 0); __builtin_amdgcn_sched_group_barrier(0x2, 2, 0); }
        }
        if constexpr (t == CVS) {
            __builtin_amdgcn_raw_buffer_store_b128((u32x4){kvp[0].x, kvp[0].y, kvp[1].x, kvp[1].y}, yrs, tid * 16, 0, 16);
            __builtin_amdgcn_raw_buffer_store_b128((u32x4){kvp[2].x, kvp[2].y, kvp[3].x, kvp[3].y}, yrs, 8192 + tid * 16, 0, 16);
        }
        __builtin_amdgcn_s_setprio(0);
        SCHED();
        if constexpr (mix) WAIT_L(0);
        SEG(sgrp, 3);
        if constexpr (t + 1 < NT2) { wait_vm<vm_wait(t)>(); SEG(sgrp, 4); BAR(); SEG(sgrp, 5); } else { if (!kh) BAR(); }
        SCHED();
    };
#define TRIP4(t) trip(std::integral_constant<int, (t)>{}); trip(std::integral_constant<int, (t) + 1>{}); trip(std::integral_constant<int, (t) + 2>{}); trip(std::integral_constant<int, (t) + 3>{})
    TRIP4(0); TRIP4(4); TRIP4(8); TRIP4(12); TRIP4(16); TRIP4(20); TRIP4(24);
    STAMP();
    if (z != kh) {
#pragma unroll
        for (int n = 0; n < 8; ++n) { const f32x4 v = st.acc[0][n]; st.acc[0][n] = st.acc[1][n]; st.acc[1][n] = v; }
    }
#pragma unroll
    for (int n = 0; n < 8; ++n) *(LAS f32x4*)(lds + wid * 8192 + lane * 16 + n * 1024) = st.acc[1][n];
    if (kh) {
        WAIT_L(0); BAR();
        const auto xs = __builtin_amdgcn_make_buffer_rsrc((void*)(ws + WS_X + (size_t)bid * 32768), 0, 32768, 0x00020000);
#pragma unroll
        for (int n = 0; n < 8; ++n)
            __builtin_amdgcn_raw_buffer_store_b128(__builtin_bit_cast(u32x4, st.acc[0][n] + *(const LAS f32x4*)(lds + wm * 8192 + lane * 16 + n * 1024)), xs,
                                                   wm * 8192 + n * 1024 + lane * 16, 0, 16);
        STAMP();
        WAIT_V(0);
        if (lane == 0) __hip_atomic_store(flags + (256 + bid * 4 + wm) * 16, 1u, RLX_AGENT);
        STAMP(); STAMP_OUT(1); SEG_OUT();
    } else {
        const int s = tm * 128 + wm * 32 + z * 16 + fr, dcol = tn * 128 + 4 * fq;
        gu32* fp = flags + ((lane < 32) ? (tn * 32 + lane) : (256 + (bid ^ 1) * 4 + wm)) * 16;
        unsigned fv = __hip_atomic_load(fp, RLX_AGENT);
        u32x2 qv[8];
#pragma unroll
        for (int n = 0; n < 8; ++n) qv[n] = *(const u32x2*)(q + (size_t)s * D_ + dcol + 16 * n);
        const float g = ((const float*)(ws + WS_G))[s];
        const auto xp = __builtin_amdgcn_make_buffer_rsrc((void*)(ws + WS_X + (size_t)(bid ^ 1) * 32768), 0, 32768, 0x00020000);
        const auto y2 = __builtin_amdgcn_make_buffer_rsrc((void*)(ws + WS_Y2), 0, 256 * S_ * 8, 0x00020000);
        WAIT_L(0); BAR();
        unsigned spins = 0;
#pragma nounroll
        while (!__all(lane >= 32 || fv == 1u) && ++spins < kSpinCap) { asm volatile("s_sleep 1" ::: "memory"); fv = __hip_atomic_load(fp, RLX_AGENT); }
        asm volatile("" ::: "memory");
        u32x2 yv[8];
#pragma unroll
        for (int n = 0; n < 8; ++n) yv[n] = __builtin_amdgcn_raw_buffer_load_b64(y2, (((dcol >> 2) + 4 * n) * S_ + s) * 8, 0, 16);
#pragma unroll
        for (int n = 0; n < 8; ++n) st.acc[0][n] += *(const LAS f32x4*)(lds + (wm + 4) * 8192 + lane * 16 + n * 1024);
        f32x4 ga[8];
#pragma unroll
        for (int n = 0; n < 8; ++n) {
            ga[n][0] = rbf(g * rbf(bf_lo(qv[n].x) * bf_lo(yv[n].x))); ga[n][1] = rbf(g * rbf(bf_hi(qv[n].x) * bf_hi(yv[n].x)));
            ga[n][2] = rbf(g * rbf(bf_lo(qv[n].y) * bf_lo(yv[n].y))); ga[n][3] = rbf(g * rbf(bf_hi(qv[n].y) * bf_hi(yv[n].y)));
        }
        STAMP();
#pragma nounroll
        while (!__all(fv == 1u) && ++spins < kSpinCap) fv = __hip_atomic_load(fp, RLX_AGENT);
        asm volatile("" ::: "memory");
        STAMP();
        u32x4 pr[8];
#pragma unroll
        for (int n = 0; n < 8; ++n) pr[n] = __builtin_amdgcn_raw_buffer_load_b128(xp, wm * 8192 + n * 1024 + lane * 16, 0, 16);
        const float omg = rbf(1.0f - g);
#pragma unroll
        for (int n = 0; n < 8; ++n) {
            const f32x4 b = st.acc[0][n] + __builtin_bit_cast(f32x4, pr[n]);
            u32x2 o;
            o.x = cvt_pk_bf16(ga[n][0] + rbf(omg * rbf(b[0])), ga[n][1] + rbf(omg * rbf(b[1])));
            o.y = cvt_pk_bf16(ga[n][2] + rbf(omg * rbf(b[2])), ga[n][3] + rbf(omg * rbf(b[3])));
            *(u32x2*)(out + (size_t)s * D_ + dcol + 16 * n) = o;
        }
        STAMP(); WAIT_V(0); STAMP(); STAMP_OUT(1); SEG_OUT();
    }
#undef STAGEB
#undef LOADA
#undef RDB
#undef CFILL
#undef RD128
#undef RD64
#undef TRIP4
}

extern "C" void kernel_launch(void* const* d_in, const int* in_sizes, int n_in, void* d_out, int out_size, void* d_ws, size_t ws_size,
                              hipStream_t stream) {
    const u16* q = (const u16*)d_in[0];
    const u16* k = (const u16*)d_in[1];
    const u16* v = (const u16*)d_in[2];
    const u16* shk = (const u16*)d_in[3];
    const u16* ssmk = (const u16*)d_in[4];
    const u16* ssmD = (const u16*)d_in[5];
    const u16* wg = (const u16*)d_in[7];
    const u16* wu = (const u16*)d_in[8];
    const u16* wd = (const u16*)d_in[9];
    const u16* gw = (const u16*)d_in[10];
    (void)hipFuncSetAttribute((const void*)k1_gemm1_prep, hipFuncAttributeMaxDynamicSharedMemorySize, K1_LDS);
    (void)hipFuncSetAttribute((const void*)k2_conv_gemm2, hipFuncAttributeMaxDynamicSharedMemorySize, K2_LDS);
    k1_gemm1_prep<<<256, 512, K1_LDS, stream>>>(q, k, v, shk, wg, wu, gw, ssmk, wd, (unsigned char*)d_ws);
    k2_conv_gemm2<<<256, 512, K2_LDS, stream>>>(q, ssmk, ssmD, wd, (unsigned char*)d_ws, (u16*)d_out);
}
```

```cpp
#include <hip/hip_runtime.h>
#include <type_traits>

#define LAS __attribute__((address_space(3)))
#define GAS __attribute__((address_space(1)))
typedef unsigned short u16;
typedef short bf16x8 __attribute__((ext_vector_type(8)));
typedef short bf16x4 __attribute__((ext_vector_type(4)));
typedef float f32x4 __attribute__((ext_vector_type(4)));
typedef float f32x2 __attribute__((ext_vector_type(2)));
typedef unsigned u32x4 __attribute__((ext_vector_type(4)));
typedef unsigned u32x2 __attribute__((ext_vector_type(2)));
typedef GAS unsigned gu32;
typedef GAS unsigned long long gu64;
#define RLX_AGENT __ATOMIC_RELAXED, __HIP_MEMORY_SCOPE_AGENT

constexpr int S_ = 2048, D_ = 1024, F_ = 3584;
constexpr int URP = 2112;
constexpr size_t WS_H = 0;
constexpr size_t WS_UR = WS_H + (size_t)S_ * F_ * 2;
constexpr size_t WS_G = WS_UR + (size_t)D_ * URP * 2;
constexpr size_t WS_Y2 = WS_G + (size_t)S_ * 4;
constexpr size_t WS_X = WS_Y2 + (size_t)256 * S_ * 8;
constexpr size_t WS_FL = WS_X + (size_t)256 * 32768;
constexpr int FL_P = 0, FL_G = 32, FL_H = 64, FL_C = 288, FL_X = 544, FL_E = 1568;
constexpr int F_LDS = 163840;
constexpr unsigned kSpinCap = 1u << 17;
typedef unsigned long long u64;

__device__ __forceinline__ unsigned cvt_pk_bf16(float lo, float hi) {
    typedef __bf16 bf16v2 __attribute__((ext_vector_type(2)));
    return __builtin_bit_cast(unsigned, __builtin_convertvector((f32x2){lo, hi}, bf16v2));
}
__device__ __forceinline__ float bf_lo(unsigned w) { return __uint_as_float(w << 16); }
__device__ __forceinline__ float bf_hi(unsigned w) { return __uint_as_float(w & 0xffff0000u); }
__device__ __forceinline__ float rbf(float x) { return bf_lo(cvt_pk_bf16(x, x)); }
__device__ __forceinline__ f32x2 rbf2(f32x2 v) { const unsigned w = cvt_pk_bf16(v.x, v.y); return (f32x2){bf_lo(w), bf_hi(w)}; }

#define GLDS16(gptr, ldsoff) __builtin_amdgcn_global_load_lds((const unsigned*)(gptr), (LAS unsigned*)(lds + (ldsoff)), 16, 0, 0)
#define GLDS16_SC1(gptr, ldsoff) __builtin_amdgcn_global_load_lds((const unsigned*)(gptr), (LAS unsigned*)(lds + (ldsoff)), 16, 0, 16)
#define LDS_B128(off) (*(const LAS bf16x8*)(lds + (off)))
#define LDS_TR(dst, vaddr, imm) asm volatile("ds_read_b64_tr_b16 %0, %1 offset:%2" : "=v"(dst) : "v"(vaddr), "i"(imm) : "memory")
#define CAT8(a, b) __builtin_shufflevector(a, b, 0, 1, 2, 3, 4, 5, 6, 7)
#define WAIT_V(n) asm volatile("s_waitcnt vmcnt(" #n ")" ::: "memory")
#define WAIT_L(n) asm volatile("s_waitcnt lgkmcnt(" #n ")" ::: "memory")
#define BAR() do { asm volatile("" ::: "memory"); __builtin_amdgcn_s_barrier(); asm volatile("" ::: "memory"); } while (0)
#define SCHED() __builtin_amdgcn_sched_barrier(0)

__device__ __forceinline__ unsigned gelu_mul2(f32x2 g, f32x2 u) {
    const f32x2 p = __builtin_elementwise_fma(g * g, (f32x2){-0.044715f * 2.3022082f, -0.044715f * 2.3022082f}, (f32x2){-2.3022082f, -2.3022082f});
    const f32x2 y = g * p;
    const f32x2 dd = (f32x2){__builtin_amdgcn_exp2f(fminf(y.x, 60.f)), __builtin_amdgcn_exp2f(fminf(y.y, 60.f))} + 1.0f;
    const float r = __builtin_amdgcn_rcpf(dd.x * dd.y);
    const f32x2 h = (g * u) * ((f32x2){dd.y, dd.x} * r);
    return cvt_pk_bf16(h.x, h.y);
}

constexpr int CV_UP = 4160, CV_HP = 4640, CV_HB = 4 * CV_UP, CV_CH = CV_HB + 2 * CV_HP;
constexpr int RB = 98304;
constexpr int rslot(int t) { return ((t + 2) % 4) * 16384; }
constexpr int NT2 = 28;
constexpr int CVP = 2;
constexpr int vm_b(int t) { return t + 4 < NT2 ? 2 : 0; }
constexpr int vm_a(int t) { return t + 3 < NT2 ? 2 : 0; }
constexpr int vm_wait(int t) { return (t > 0 ? vm_b(t - 1) + vm_a(t - 1) : 2) + vm_b(t) + vm_a(t); }
template <int N> __device__ __forceinline__ void wait_vm() { asm volatile("s_waitcnt vmcnt(%0)" :: "n"(N) : "memory"); }
__device__ constexpr int kSlA[3][12] = {{0,0,0,0,1,1,1,1,2,2,2,2}, {0,0,0,0,1,1,1,2,2,3,3,3}, {3,3,4,4,4,4,5,5,5,6,6,7}};
__device__ constexpr int kSlB[3][12] = {{0,1,2,3,0,1,2,3,0,1,2,3}, {4,5,6,7,4,5,6,4,5,4,3,2}, {0,1,0,1,2,3,0,1,2,0,1,0}};

struct K2State {
    f32x4 acc[2][8];
    bf16x8 A[4][2];
    bf16x4 B[2][16];
};

__device__ __forceinline__ void take_epoch(u64& e) {
    asm volatile("" : "+v"(e) :: "memory");
    e += 1;
    e = ((u64)(unsigned)__builtin_amdgcn_readfirstlane((unsigned)(e >> 32)) << 32) | (unsigned)__builtin_amdgcn_readfirstlane((unsigned)e);
}
__device__ __forceinline__ void wait_flags(gu64* fp, bool use, u64 epoch) {
    unsigned spins = 0;
#pragma nounroll
    while (!__all(!use || __hip_atomic_load(fp, RLX_AGENT) == epoch) && ++spins < kSpinCap) __builtin_amdgcn_s_sleep(2);
    asm volatile("" ::: "memory");
}
template <int PART> __device__ __forceinline__ void phase2_fill(LAS unsigned char* lds, const u16* ssmk, const u16* wd, unsigned char* ws, u32x2 h03 = (u32x2){0u, 0u}) {
    const int tid = threadIdx.x, wid = __builtin_amdgcn_readfirstlane(tid >> 6), lane = tid & 63;
    const int bid = blockIdx.x, xg = bid & 7, l = bid >> 3, z = xg & 1, tn = 4 * (xg >> 2) + (l >> 3), kh = wid >> 2, wm = wid & 3;
    const int d = bid * 4 + wm, CB = wm * CV_CH;
    if constexpr (PART == 0) {
        const char* cB = (const char*)wd + (size_t)z * 1792 * 2048 + (size_t)tn * 256;
#pragma unroll
        for (int i = 0; i < 4; ++i) { const int kr = 32 * kh + (wm + 4 * (i & 1)) * 4 + (lane >> 4), ch = (lane & 15) ^ (2 * (kr & 7));
            GLDS16(cB + (size_t)(i >> 1) * 64 * 2048 + kr * 2048 + ch * 16, RB + rslot(i >> 1) + kh * 8192 + wm * 1024 + (i & 1) * 4096); }
    } else if constexpr (PART == 1) {
        const auto hrs = __builtin_amdgcn_make_buffer_rsrc((void*)ssmk, 0, D_ * S_ * 2, 0x00020000);
#pragma unroll
        for (int i = 0; i < 4; ++i)
            __builtin_amdgcn_raw_ptr_buffer_load_lds(hrs, (LAS void*)(lds + CB + CV_HB + kh * CV_HP + (29 - kh) * 16 + i * 1024), 16,
                                                     d * (S_ * 2) + (1 - kh) * 8 + lane * 16 + i * 1024, 0, 0, 0);
    } else if constexpr (PART == 4) {
        const int e = lane - 28;
        const unsigned pa = (unsigned)(size_t)lds + CB + (lane < 28 ? CV_HB + kh * CV_HP + lane * 16 : lane < 34 ? (2 * kh + e / 3) * CV_UP + 4096 + (e % 3) * 16 : CV_HB + 28 * 16);
        const u32x4 pd = {0u, 0u, lane == 34 ? h03.x : 0u, lane == 34 ? h03.y : 0u};
        if (lane < 34 + (1 - kh)) asm volatile("ds_write_b128 %0, %1" :: "v"(pa), "v"(pd) : "memory");
    } else {
        const char* ur = (const char*)(ws + WS_UR) + (size_t)d * URP * 2 + (3 - 2 * kh - (PART - 2)) * 2 + lane * 16;
#pragma unroll
        for (int i = 0; i < 4; ++i) GLDS16_SC1(ur + i * 1024, CB + (2 * kh + (PART - 2)) * CV_UP + i * 1024);
    }
}
#define POLL_KV(pv) asm volatile("global_load_dwordx2 %0, %1, off sc1" : "=v"(pv) : "v"((gu64*)(ws + WS_FL) + (FL_P + (lane & 31)) * 8) : "memory")

__device__ __forceinline__ bool prep_block(LAS unsigned char* lds, int j, const u16* x, const u16* kk, const u16* vv, const u16* shk, const u16* gw,
                                           const u16* wg, const u16* wu, const u16* ssmk, const u16* wd, unsigned char* ws, u64& epoch, u32x2 h03) {
    const int tid = threadIdx.x, wid = __builtin_amdgcn_readfirstlane(tid >> 6), lane = tid & 63;
    const unsigned ldsw = (unsigned)wid * 1024u, scr = 131072 + ldsw;
    gu64* flags = (gu64*)(ws + WS_FL);
    const auto urs = __builtin_amdgcn_make_buffer_rsrc((void*)(ws + WS_UR), 0, D_ * URP * 2, 0x00020000);
    if (tid >= 256) { const int t = tid - 256; __builtin_amdgcn_raw_buffer_store_b128((u32x4){0u, 0u, 0u, 0u}, urs, ((j * 32 + (t >> 3)) * URP + 2048) * 2 + (t & 7) * 16, 0, 16); }
    {
        unsigned long long pc1;
        asm volatile("s_getpc_b64 s[40:41]\n\ts_add_u32 s40, s40, _Z7k_fusedPKtS0_S0_S0_S0_S0_S0_S0_S0_S0_PhPt@rel32@lo+4\n\ts_addc_u32 s41, s41, _Z7k_fusedPKtS0_S0_S0_S0_S0_S0_S0_S0_S0_PhPt@rel32@hi+12\n\ts_mov_b64 %0, s[40:41]" : "=s"(pc1) :: "s40", "s41", "scc");
        const char* c1 = (const char*)pc1 + ldsw + lane * 16;
        GLDS16(c1, scr); GLDS16(c1 + 8192, scr); GLDS16(c1 + 16384, scr); GLDS16(c1 + 24576, scr); GLDS16(c1 + 32768, scr);
    }
    const int cg = tid & 127, rg = tid >> 7;
    {
        const int s0 = 64 * j + 16 * rg;
        u32x4 kr[19], vr[16], wr4[4];
#define LD16(dst, ptr) asm volatile("global_load_dwordx4 %0, %1, off" : "=v"(dst) : "v"(ptr) : "memory")
#pragma unroll
        for (int i = 0; i < 4; ++i) LD16(wr4[i], shk + cg * 32 + i * 8);
#pragma unroll
        for (int i = 0; i < 19; ++i) { const int s = s0 - 3 + i; LD16(kr[i], kk + (size_t)(s < 0 ? 0 : s) * D_ + cg * 8); }
#pragma unroll
        for (int i = 0; i < 16; ++i) LD16(vr[i], vv + (size_t)(s0 + i) * D_ + cg * 8);
#undef LD16
        {
            constexpr int TW0 = 11;
            const char* w = (const char*)((wid < 4) ? wg : wu) + (size_t)j * 14336 + (wid & 3) * 1024 + lane * 16 + (size_t)TW0 * 64 * 7168;
#pragma unroll
            for (int t = 0; t < 4; ++t) {
                const char* p = w + (size_t)t * 64 * 7168;
                GLDS16(p, scr); GLDS16(p + 4096, scr); GLDS16(p + 8192, scr); GLDS16(p + ((wid & 3) < 2 ? 12288 : 0), scr);
            }
        }
        asm volatile("s_waitcnt vmcnt(16)" : "+v"(wr4[0]), "+v"(wr4[1]), "+v"(wr4[2]), "+v"(wr4[3]), "+v"(kr[0]), "+v"(kr[1]), "+v"(kr[2]), "+v"(kr[3]), "+v"(kr[4]), "+v"(kr[5]),
                     "+v"(kr[6]), "+v"(kr[7]), "+v"(kr[8]), "+v"(kr[9]), "+v"(kr[10]), "+v"(kr[11]), "+v"(kr[12]), "+v"(kr[13]), "+v"(kr[14]), "+v"(kr[15]), "+v"(kr[16]),
                     "+v"(kr[17]), "+v"(kr[18]) :: "memory");
        asm volatile("" : "+v"(vr[0]), "+v"(vr[1]), "+v"(vr[2]), "+v"(vr[3]), "+v"(vr[4]), "+v"(vr[5]), "+v"(vr[6]), "+v"(vr[7]), "+v"(vr[8]), "+v"(vr[9]), "+v"(vr[10]),
                     "+v"(vr[11]), "+v"(vr[12]), "+v"(vr[13]), "+v"(vr[14]), "+v"(vr[15]) :: "memory");
        if (s0 == 0) kr[0] = kr[1] = kr[2] = (u32x4){0u, 0u, 0u, 0u};
#pragma unroll
        for (int cp = 0; cp < 4; ++cp) {
            const f32x2 w0 = {bf_lo(wr4[cp].x), bf_lo(wr4[cp].z)}, w1 = {bf_hi(wr4[cp].x), bf_hi(wr4[cp].z)};
            const f32x2 w2 = {bf_lo(wr4[cp].y), bf_lo(wr4[cp].w)}, w3 = {bf_hi(wr4[cp].y), bf_hi(wr4[cp].w)};
            f32x2 kf[19], o[16];
#pragma unroll
            for (int i = 0; i < 19; ++i) kf[i] = (f32x2){bf_lo(kr[i][cp]), bf_hi(kr[i][cp])};
#pragma unroll
            for (int i = 0; i < 16; ++i) {
                f32x2 a = kf[i] * w3; a = __builtin_elementwise_fma(kf[i + 1], w2, a); a = __builtin_elementwise_fma(kf[i + 2], w1, a); a = __builtin_elementwise_fma(kf[i + 3], w0, a);
                o[i] = a * (f32x2){bf_lo(vr[i][cp]), bf_hi(vr[i][cp])};
            }
#pragma unroll
            for (int hh = 0; hh < 2; ++hh)
#pragma unroll
                for (int e = 0; e < 2; ++e) {
                    u32x4 pk; pk.x = cvt_pk_bf16(o[8 * hh + 7][e], o[8 * hh + 6][e]); pk.y = cvt_pk_bf16(o[8 * hh + 5][e], o[8 * hh + 4][e]);
                    pk.z = cvt_pk_bf16(o[8 * hh + 3][e], o[8 * hh + 2][e]); pk.w = cvt_pk_bf16(o[8 * hh + 1][e], o[8 * hh][e]);
                    const int chunk = 7 - (2 * rg + hh);
                    asm volatile("ds_write_b128 %0, %1 offset:%2" :: "v"((unsigned)(size_t)lds + cg * 1024 + ((chunk ^ (cg & 7)) * 16)), "v"(pk), "i"((2 * cp + e) * 128) : "memory");
                }
        }
    }
    WAIT_L(0); BAR();
    const int xb = (1984 - 64 * j) * 2;
#pragma unroll 4
    for (int it = 0; it < 16; ++it) {
        const int ch = it * 64 + (tid >> 3), c = tid & 7;
        const u32x4 t = *(const LAS u32x4*)(lds + ch * 128 + ((c ^ ((ch >> 3) & 7)) * 16));
        __builtin_amdgcn_raw_buffer_store_b128(t, urs, ch * (URP * 2) + xb + c * 16, 0, 16);
    }
    u32x4 g0, g1, a0[8], a1[8];
#define LD16(dst, ptr) asm volatile("global_load_dwordx4 %0, %1, off" : "=v"(dst) : "v"(ptr) : "memory")
    LD16(g0, gw + lane * 8); LD16(g1, gw + 512 + lane * 8);
#pragma unroll
    for (int r = 0; r < 8; ++r) { const u16* xr = x + (size_t)(64 * j + wid * 8 + r) * D_ + lane * 8; LD16(a0[r], xr); LD16(a1[r], xr + 512); }
#undef LD16
    const char* p1 = (const char*)ssmk + (size_t)j * 131072 + ldsw + lane * 16;
    const char* p2 = (const char*)wd + (size_t)j * 229376 + ldsw + lane * 16;
#pragma unroll
    for (int i = 0; i < 12; ++i) GLDS16(p2 + i * 8192, scr);
    WAIT_V(30); WAIT_L(0); BAR();
    take_epoch(epoch);
    if (tid == 0) __hip_atomic_store(flags + (FL_P + j) * 8, epoch, RLX_AGENT);
    asm volatile("s_waitcnt vmcnt(12)" : "+v"(g0), "+v"(g1), "+v"(a0[0]), "+v"(a0[1]), "+v"(a0[2]), "+v"(a0[3]), "+v"(a0[4]), "+v"(a0[5]), "+v"(a0[6]), "+v"(a0[7]),
                 "+v"(a1[0]), "+v"(a1[1]), "+v"(a1[2]), "+v"(a1[3]), "+v"(a1[4]), "+v"(a1[5]), "+v"(a1[6]), "+v"(a1[7]) :: "memory");
#pragma unroll
    for (int i = 12; i < 28; ++i) GLDS16(p2 + i * 8192, scr);
#pragma unroll
    for (int i = 0; i < 16; ++i) GLDS16(p1 + i * 8192, scr);
    {
        f32x2 gf[8];
#pragma unroll
        for (int i = 0; i < 4; ++i) { gf[i] = (f32x2){bf_lo(g0[i]), bf_hi(g0[i])}; gf[4 + i] = (f32x2){bf_lo(g1[i]), bf_hi(g1[i])}; }
        float acc[8];
#pragma unroll
        for (int r = 0; r < 8; ++r) { f32x2 t = {0.f, 0.f};
#pragma unroll
            for (int i = 0; i < 4; ++i) {
                t += rbf2((f32x2){bf_lo(a0[r][i]), bf_hi(a0[r][i])} * gf[i]);
                t += rbf2((f32x2){bf_lo(a1[r][i]), bf_hi(a1[r][i])} * gf[4 + i]);
            }
            acc[r] = t.x + t.y; }
#pragma unroll
        for (int r = 0; r < 4; ++r) { const bool up = lane & 32; const float keep = up ? acc[4 + r] : acc[r], give = up ? acc[r] : acc[4 + r]; acc[r] = keep + __shfl_xor(give, 32); }
#pragma unroll
        for (int r = 0; r < 2; ++r) { const bool up = lane & 16; const float keep = up ? acc[2 + r] : acc[r], give = up ? acc[r] : acc[2 + r]; acc[r] = keep + __shfl_xor(give, 16); }
        { const bool up = lane & 8; const float keep = up ? acc[1] : acc[0], give = up ? acc[0] : acc[1]; acc[0] = keep + __shfl_xor(give, 8); }
        float z = acc[0];
        z += __shfl_xor(z, 4); z += __shfl_xor(z, 2); z += __shfl_xor(z, 1);
        if ((lane & 7) == 0) __hip_atomic_store((gu32*)(ws + WS_G) + 64 * j + wid * 8 + (lane >> 3), __float_as_uint(rbf(1.0f / (1.0f + __expf(-rbf(z))))), RLX_AGENT);
    }
    WAIT_V(0); BAR();
    u64 pv; POLL_KV(pv);
    phase2_fill<0>(lds, ssmk, wd, ws); phase2_fill<1>(lds, ssmk, wd, ws); phase2_fill<4>(lds, ssmk, wd, ws, h03);
    asm volatile("s_waitcnt vmcnt(8)" : "+v"(pv) :: "memory");
    const bool kvr = __all(pv == epoch);
    if (kvr) { phase2_fill<2>(lds, ssmk, wd, ws); phase2_fill<3>(lds, ssmk, wd, ws); }
    WAIT_V(0);
    return kvr;
}

__device__ __forceinline__ bool gemm1_block(LAS unsigned char* lds, int xg, int l, const u16* x, const u16* wg, const u16* wu, const u16* ssmk, const u16* wd,
                                            unsigned char* ws, u64& epoch, u32x2 h03) {
    const int pm = 4 * (xg & 1) + (l & 3), pn = 7 * (xg >> 1) + (l >> 2);
    const int tid = threadIdx.x, wid = __builtin_amdgcn_readfirstlane(tid >> 6), lane = tid & 63, wr = wid >> 2, wc = wid & 3, fr = lane & 15, fq = lane >> 4;
    constexpr int HTB = 16384;
    unsigned voffA[2], voffB[2];
#pragma unroll
    for (int i = 0; i < 2; ++i) {
        const int piece = wid + 8 * i;
        { const int row = piece * 8 + (lane >> 3), ch = (lane & 7) ^ (lane >> 3); voffA[i] = (unsigned)(row * 2048 + ch * 16); }
        { const int kr = piece * 4 + (lane >> 4), fs = (kr & 3) | (((kr >> 3) & 1) << 2), ch = (lane & 15) ^ (2 * fs); voffB[i] = (unsigned)(kr * (F_ * 2) + ch * 16); }
    }
    const unsigned ldsw = (unsigned)wid * 1024u;
    const int aoff0 = ((wr * 64 + fr) >> 3) * 1024 + (fr & 7) * 128 + ((fq ^ (fr & 7)) * 16), aoff1 = aoff0 ^ 64;
    const int tq = (lane & 15) >> 2, tp = lane & 3;
    const unsigned boff0 = (unsigned)(size_t)lds + 4 * HTB + (8 * fq + tq) * 256 + (((4 * wc + (tp >> 1)) ^ (2 * (tq | ((fq & 1) << 2)))) * 16) + 8 * (tp & 1), boff1 = boff0 ^ 32;
    const char* cA = (const char*)x + (size_t)pm * 256 * 2048;
    const char* cG = (const char*)wg + (size_t)pn * 256;
    const char* cU = (const char*)wu + (size_t)pn * 256;
    constexpr size_t kA = 128, kB = (size_t)64 * F_ * 2, hA = (size_t)128 * 2048;
#define SA(b, h) (((b) * 2 + (h)) * HTB)
#define SB(b, h) ((4 + (b) * 2 + (h)) * HTB)
#define STAGE(bufoff, gbase, voff) do { _Pragma("unroll") for (int _i = 0; _i < 2; ++_i) GLDS16((gbase) + (voff)[_i], (bufoff) + ldsw + _i * 8192); } while (0)
#define LDA(dst, b, h) do { _Pragma("unroll") for (int m = 0; m < 4; ++m) { dst[m][0] = LDS_B128(SA(b, h) + aoff0 + m * 2048); dst[m][1] = LDS_B128(SA(b, h) + aoff1 + m * 2048); } } while (0)
#define LDB(dst, b, h) do { \
        LDS_TR(dst[0], boff0, ((b) * 2 + (h)) * HTB); LDS_TR(dst[1], boff0, ((b) * 2 + (h)) * HTB + 1024); \
        LDS_TR(dst[2], boff0, ((b) * 2 + (h)) * HTB + 8192); LDS_TR(dst[3], boff0, ((b) * 2 + (h)) * HTB + 9216); \
        LDS_TR(dst[4], boff1, ((b) * 2 + (h)) * HTB); LDS_TR(dst[5], boff1, ((b) * 2 + (h)) * HTB + 1024); \
        LDS_TR(dst[6], boff1, ((b) * 2 + (h)) * HTB + 8192); LDS_TR(dst[7], boff1, ((b) * 2 + (h)) * HTB + 9216); } while (0)
#define WAIT_B(B) asm volatile("s_waitcnt lgkmcnt(0)" : "+v"(B[0]), "+v"(B[1]), "+v"(B[2]), "+v"(B[3]), "+v"(B[4]), "+v"(B[5]), "+v"(B[6]), "+v"(B[7]) :: "memory")
#define MMA(ai, bj, At, Bt) do { __builtin_amdgcn_s_setprio(1); _Pragma("unroll") for (int m = 0; m < 4; ++m) _Pragma("unroll") for (int n = 0; n < 2; ++n) _Pragma("unroll") for (int k = 0; k < 2; ++k) \
        acc[ai][bj][m][n] = __builtin_amdgcn_mfma_f32_16x16x32_bf16(CAT8(Bt[4 * n + 2 * k], Bt[4 * n + 2 * k + 1]), At[m][k], acc[ai][bj][m][n], 0, 0, 0); __builtin_amdgcn_s_setprio(0); } while (0)
    f32x4 acc[2][2][4][2];
#pragma unroll
    for (int a = 0; a < 2; ++a)
#pragma unroll
        for (int b = 0; b < 2; ++b)
#pragma unroll
            for (int m = 0; m < 4; ++m)
#pragma unroll
                for (int n = 0; n < 2; ++n) acc[a][b][m][n] = (f32x4){0.f, 0.f, 0.f, 0.f};
    bf16x8 At[4][2]; bf16x4 B0[8], B1[8];
    constexpr int nt = D_ / 64;
    STAGE(SB(0, 0), cG, voffB); STAGE(SA(0, 0), cA, voffA); STAGE(SB(0, 1), cU, voffB); STAGE(SA(0, 1), cA + hA, voffA);
    if (wr == 1) BAR();
    WAIT_V(4); BAR();
    take_epoch(epoch);
    STAGE(SB(1, 0), cG + kB, voffB); STAGE(SA(1, 0), cA + kA, voffA); STAGE(SB(1, 1), cU + kB, voffB);
    WAIT_V(6); BAR();
#pragma unroll 1
    for (int t = 0; t < nt - 2; t += 2) {
        const char* a1 = cA + (size_t)(t + 1) * kA;
        const char* a2 = a1 + kA; const char* g2 = cG + (size_t)(t + 2) * kB; const char* u2 = cU + (size_t)(t + 2) * kB;
        const char* a3 = a2 + kA; const char* g3 = g2 + kB; const char* u3 = u2 + kB;
        LDB(B0, 0, 0); SCHED(); LDA(At, 0, 0); STAGE(SA(1, 1), a1 + hA, voffA);
        WAIT_L(8); BAR(); WAIT_B(B0); MMA(0, 0, At, B0); BAR(); SCHED();
        LDB(B1, 0, 1); STAGE(SB(0, 0), g2, voffB);
        BAR(); WAIT_B(B1); MMA(0, 1, At, B1); BAR();
        LDA(At, 0, 1); STAGE(SA(0, 0), a2, voffA);
        BAR(); WAIT_L(0); MMA(1, 0, At, B0); BAR(); SCHED();
        STAGE(SB(0, 1), u2, voffB);
        WAIT_V(6); BAR(); MMA(1, 1, At, B1); BAR();
        LDB(B0, 1, 0); SCHED(); LDA(At, 1, 0); STAGE(SA(0, 1), a2 + hA, voffA);
        WAIT_L(8); BAR(); WAIT_B(B0); MMA(0, 0, At, B0); BAR(); SCHED();
        LDB(B1, 1, 1); STAGE(SB(1, 0), g3, voffB);
        BAR(); WAIT_B(B1); MMA(0, 1, At, B1); BAR();
        LDA(At, 1, 1); STAGE(SA(1, 0), a3, voffA);
        BAR(); WAIT_L(0); MMA(1, 0, At, B0); BAR(); SCHED();
        STAGE(SB(1, 1), u3, voffB);
        WAIT_V(6); BAR(); MMA(1, 1, At, B1); BAR();
    }
    u64 pv; bool kvr;
    {
        LDB(B0, 0, 0); SCHED(); LDA(At, 0, 0); STAGE(SA(1, 1), cA + (size_t)(nt - 1) * kA + hA, voffA);
        WAIT_L(8); BAR(); WAIT_B(B0); MMA(0, 0, At, B0); BAR(); SCHED();
        LDB(B1, 0, 1); POLL_KV(pv); phase2_fill<0>(lds, ssmk, wd, ws);
        BAR(); WAIT_B(B1); MMA(0, 1, At, B1); BAR();
        LDA(At, 0, 1);
        BAR(); WAIT_L(0); MMA(1, 0, At, B0); BAR(); SCHED();
        WAIT_V(5); BAR(); MMA(1, 1, At, B1); BAR();
        LDB(B0, 1, 0); SCHED(); LDA(At, 1, 0);
        asm volatile("s_waitcnt vmcnt(0)" : "+v"(pv) :: "memory");
        kvr = __all(pv == epoch);
        if ((wc & 1) == 0) phase2_fill<1>(lds, ssmk, wd, ws);
        BAR(); WAIT_B(B0); MMA(0, 0, At, B0); BAR(); SCHED();
        LDB(B1, 1, 1);
        if (kvr && (wc == 0 || wc == 3)) phase2_fill<2>(lds, ssmk, wd, ws);
        BAR(); WAIT_B(B1); MMA(0, 1, At, B1); BAR();
        LDA(At, 1, 1);
        if (kvr && (wc == 0 || wc == 3)) phase2_fill<3>(lds, ssmk, wd, ws);
        BAR(); WAIT_L(0); MMA(1, 0, At, B0); MMA(1, 1, At, B1); BAR();
    }
    if (wr == 0) BAR();
    phase2_fill<4>(lds, ssmk, wd, ws, h03);
    if (wc & 1) phase2_fill<1>(lds, ssmk, wd, ws);
    const bool late = kvr && (wc == 1 || wc == 2);
    {
        const auto hrs = __builtin_amdgcn_make_buffer_rsrc((void*)(ws + WS_H), 0, S_ * F_ * 2, 0x00020000);
        const int hp = ((pm * 16 + wr * 4) * 112 + pn * 4 + wc) * 1024 + lane * 16;
#pragma unroll
        for (int ai = 0; ai < 2; ++ai)
#pragma unroll
            for (int m = 0; m < 4; ++m) {
                const f32x4 g0 = acc[ai][0][m][0], g1 = acc[ai][0][m][1], u0 = acc[ai][1][m][0], u1 = acc[ai][1][m][1];
                u32x4 o;
                o.x = gelu_mul2(g0.xy, u0.xy); o.y = gelu_mul2(g0.zw, u0.zw); o.z = gelu_mul2(g1.xy, u1.xy); o.w = gelu_mul2(g1.zw, u1.zw);
                __builtin_amdgcn_raw_buffer_store_b128(o, hrs, hp + (ai * 8 + m) * 112 * 1024, 0, 16);
                if (ai == 0 && m == 0) { if (late) phase2_fill<2>(lds, ssmk, wd, ws); }
                if (ai == 0 && m == 1) { if (late) phase2_fill<3>(lds, ssmk, wd, ws); }
                SCHED();
            }
    }
    WAIT_V(6);
    return kvr;
#undef SA
#undef SB
#undef STAGE
#undef LDA
#undef LDB
#undef MMA
#undef WAIT_B
}

__global__ __launch_bounds__(512, 2) void k_fused(const u16* __restrict__ q, const u16* __restrict__ kk, const u16* __restrict__ vv, const u16* __restrict__ shk,
                                                  const u16* __restrict__ ssmk, const u16* __restrict__ ssmD, const u16* __restrict__ wg, const u16* __restrict__ wu,
                                                  const u16* __restrict__ wd, const u16* __restrict__ gw, unsigned char* ws, u16* __restrict__ out) {
    extern __shared__ __attribute__((aligned(16))) unsigned char smem[];
    LAS unsigned char* lds = (LAS unsigned char*)smem;
    gu64* flags = (gu64*)(ws + WS_FL);
    const int d_ = blockIdx.x * 4 + (__builtin_amdgcn_readfirstlane(threadIdx.x >> 6) & 3);
    const u32x2 h03 = *(const u32x2*)(ssmk + (size_t)d_ * S_);
    const unsigned dpair = ((const unsigned*)ssmD)[d_ >> 1];
    u64 epoch;
    asm volatile("global_load_dwordx2 %0, %1, off sc1" : "=v"(epoch) : "v"(flags + FL_E * 8) : "memory");
    bool kvr;
    if ((blockIdx.x >> 3) >= 28) kvr = prep_block(lds, (blockIdx.x & 7) * 4 + ((blockIdx.x >> 3) - 28), q, kk, vv, shk, gw, wg, wu, ssmk, wd, ws, epoch, h03);
    else kvr = gemm1_block(lds, blockIdx.x & 7, blockIdx.x >> 3, q, wg, wu, ssmk, wd, ws, epoch, h03);
    asm volatile("ds_write_b32 %0, %1" :: "v"((unsigned)(size_t)lds + 4 * CV_CH + (threadIdx.x >> 6) * 4), "v"((unsigned)kvr) : "memory");
    WAIT_L(0); BAR();
    const unsigned lbase = (unsigned)(size_t)lds;
    int tid = threadIdx.x; asm volatile("" : "+v"(tid));
    const int wid = __builtin_amdgcn_readfirstlane(tid >> 6), lane = tid & 63, fr = lane & 15, fq = lane >> 4;
    const int bid = blockIdx.x, xg = bid & 7, l = bid >> 3;
    const int z = xg & 1, tm = 8 * ((xg >> 1) & 1) + (l & 7), tn = 4 * (xg >> 2) + (l >> 3);
    const int kh = wid >> 2, wm = wid & 3;
    unsigned voffB[2];
#pragma unroll
    for (int i = 0; i < 2; ++i) { const int kr = 32 * kh + (wm + 4 * i) * 4 + (lane >> 4), ch = (lane & 15) ^ (2 * (kr & 7)); voffB[i] = (unsigned)(kr * 2048 + ch * 16); }
    const unsigned ldsw = (unsigned)(kh * 8192 + wm * 1024), voffA = (unsigned)lane * 16u;
    const char* cA0 = (const char*)(ws + WS_H) + (unsigned)__builtin_amdgcn_readfirstlane(((tm * 8 + wm * 2) * 112 + z * 56 + kh) * 1024);
    const char* cA1 = cA0 + 112 * 1024;
    const char* cB = (const char*)wd + (size_t)z * 1792 * 2048 + (size_t)tn * 256;
    constexpr size_t kB = (size_t)64 * 2048;
    K2State st;
#define STAGEB(t) do { const char* _b = cB + (size_t)(t) * kB; GLDS16(_b + voffB[0], RB + rslot(t) + ldsw); GLDS16(_b + voffB[1], RB + rslot(t) + ldsw + 4096); } while (0)
#define LOADA(t) do { asm volatile("global_load_dwordx4 %0, %1, %2 sc1" : "=v"(st.A[(t) & 3][0]) : "v"(voffA), "s"(cA0 + (size_t)(t) * 2048) : "memory"); \
        asm volatile("global_load_dwordx4 %0, %1, %2 sc1" : "=v"(st.A[(t) & 3][1]) : "v"(voffA), "s"(cA1 + (size_t)(t) * 2048) : "memory"); } while (0)
    const int ch = wid & 3, hf = wid >> 2, d = bid * 4 + ch, CB = ch * CV_CH;
    const float dval = (d & 1) ? bf_hi(dpair) : bf_lo(dpair);
    const int tq = fr >> 2, tp4 = lane & 3;
    unsigned boff[8];
    { const unsigned b0 = lbase + RB + (32 * kh + 4 * fq + tq) * 256 + 8 * (tp4 & 1) + (((tp4 >> 1) ^ (2 * ((4 * fq + tq) & 7))) * 16);
#pragma unroll
      for (int n = 0; n < 8; ++n) boff[n] = b0 ^ (32 * n); }
    const unsigned ua = lbase + CB + (fr & 3) * CV_UP + 2 * (2040 - 8 * (fr >> 2) + 8 * fq) - 64 * 59 - 256 * hf;
    const unsigned ha = lbase + CB + CV_HB + (fr & 1) * CV_HP + 2 * (224 + 32 * (fr >> 1) + 8 * fq) - 192 - 256 * hf;
#pragma unroll
    for (int m = 0; m < 2; ++m)
#pragma unroll
        for (int n = 0; n < 8; ++n) st.acc[m][n] = (f32x4){0.f, 0.f, 0.f, 0.f};
#define RDB(t, n) do { LDS_TR(st.B[(t) & 1][2 * (n)], boff[n], rslot(t)); LDS_TR(st.B[(t) & 1][2 * (n) + 1], boff[n], rslot(t) + 4096); } while (0)
#define RD64(dst, vaddr, imm) asm volatile("ds_read_b64 %0, %1 offset:%2" : "=v"(dst) : "v"(vaddr), "i"(imm) : "memory")
    const int sl0 = 32 * (fr >> 1) + 4 * (fr & 1) + 8 * fq;
    const unsigned ypa = lbase + CB + CV_HB + (1 - hf) * CV_HP + lane * 16;
    const unsigned kva = lbase + CB + 3 * CV_UP + 2 * (2044 - 1024 * hf - sl0) - 1536;
    const unsigned ypw = lbase + CB + CV_HB + hf * CV_HP + lane * 16;
    const unsigned twa = lbase + 2 * hf * CV_CH + sl0 * 8 + (fr >> 1) * 8 + ch * 2;
    const unsigned tra = lbase + (tid >> 8) * CV_CH + (tid & 255) * 16 + ((tid & 255) >> 4) * 8;
#define RD128(dst, vaddr, imm) asm volatile("ds_read_b128 %0, %1 offset:%2" : "=v"(dst) : "v"(vaddr), "i"(imm) : "memory")
    const auto yrs = __builtin_amdgcn_make_buffer_rsrc((void*)(ws + WS_Y2 + (size_t)bid * 16384), 0, 16384, 0x00020000);
    gu64* hfp = flags + (FL_H + (tm >> 1) * 28 + z * 14 + (lane < 14 ? lane : 0)) * 8;
    {
        f32x4 cacc[8];
#pragma unroll
        for (int n = 0; n < 8; ++n) cacc[n] = (f32x4){0.f, 0.f, 0.f, 0.f};
        bf16x8 Ua[8], Ha[8], Ub[8], Hb[8];
#define conv_issue(U, H, cc) do { _Pragma("unroll") for (int a = 0; a < 8; ++a) { RD128(U[a], ua, 64 * (59 - 8 * a - (cc))); RD128(H[a], ha, 64 * (8 * a - (cc) + 3)); } } while (0)
#define conv_wait(U, H) asm volatile("s_waitcnt lgkmcnt(0)" : "+v"(U[0]), "+v"(U[1]), "+v"(U[2]), "+v"(U[3]), "+v"(U[4]), "+v"(U[5]), "+v"(U[6]), "+v"(U[7]), \
                     "+v"(H[0]), "+v"(H[1]), "+v"(H[2]), "+v"(H[3]), "+v"(H[4]), "+v"(H[5]), "+v"(H[6]), "+v"(H[7]) :: "memory")
#define conv_mma(U, H) do { SCHED(); _Pragma("unroll") for (int sl = 0; sl < 3; ++sl) _Pragma("unroll") for (int i = 0; i < 12; ++i) \
            cacc[kSlA[sl][i] + kSlB[sl][i]] = __builtin_amdgcn_mfma_f32_16x16x32_bf16(U[kSlA[sl][i]], H[kSlB[sl][i]], cacc[kSlA[sl][i] + kSlB[sl][i]], 0, 0, 0); \
        SCHED(); } while (0)
        u32x4 kr0, kr1;
        RD128(kr0, lbase + 4 * CV_CH, 0); RD128(kr1, lbase + 4 * CV_CH, 16);
        asm volatile("s_waitcnt lgkmcnt(0)" : "+v"(kr0), "+v"(kr1) :: "memory");
        const bool allkv = __builtin_amdgcn_readfirstlane(kr0.x & kr0.y & kr0.z & kr0.w & kr1.x & kr1.y & kr1.z & kr1.w) != 0;
        gu64* myflag = flags + (l >= 28 ? FL_G + xg * 4 + (l - 28) : FL_H + (4 * (xg & 1) + (l & 3)) * 28 + 7 * (xg >> 1) + (l >> 2)) * 8;
        if (!allkv) {
            WAIT_V(0); BAR();
            if (tid == 0) __hip_atomic_store(myflag, epoch, RLX_AGENT);
            if (!kvr) { wait_flags(flags + (FL_P + (lane & 31)) * 8, true, epoch); phase2_fill<2>(lds, ssmk, wd, ws); phase2_fill<3>(lds, ssmk, wd, ws); WAIT_V(0); }
            BAR();
        }
        conv_issue(Ua, Ha, 0); conv_wait(Ua, Ha);
        conv_issue(Ub, Hb, 1); conv_mma(Ua, Ha); conv_wait(Ub, Hb);
        WAIT_V(0); BAR();
        if (tid == 0) __hip_atomic_store(myflag, epoch, RLX_AGENT);
        conv_issue(Ua, Ha, 2); conv_mma(Ub, Hb); conv_wait(Ua, Ha);
        u64 hv;
        asm volatile("global_load_dwordx2 %0, %1, off sc1" : "=v"(hv) : "v"(hfp) : "memory");
        conv_issue(Ub, Hb, 3); conv_mma(Ua, Ha); conv_wait(Ub, Hb);
        asm volatile("s_waitcnt vmcnt(0)" : "+v"(hv) :: "memory");
        if (!__all(hv == epoch)) wait_flags(hfp, true, epoch);
        asm volatile("" ::: "memory");
        STAGEB(3); LOADA(0); LOADA(1); LOADA(2);
        BAR();
        conv_mma(Ub, Hb);
        asm volatile("s_nop 15" ::: "memory");
        SCHED();
        if (hf) {
#pragma unroll
            for (int i = 0; i < 4; ++i) { const f32x4 v = cacc[i]; cacc[i] = cacc[4 + i]; cacc[4 + i] = v; }
        }
        u32x2 kvp[4];
#pragma unroll
        for (int i = 0; i < 4; ++i) {
            asm volatile("ds_write_b128 %0, %1 offset:%2" :: "v"(ypw), "v"(cacc[4 + i]), "i"(i * 1024) : "memory");
            RD64(kvp[i], kva, 1536 - i * 512);
        }
        WAIT_L(0); BAR();
        f32x4 yp[4];
#pragma unroll
        for (int i = 0; i < 4; ++i) RD128(yp[i], ypa, i * 1024);
        asm volatile("s_waitcnt lgkmcnt(0)" : "+v"(yp[0]), "+v"(yp[1]), "+v"(yp[2]), "+v"(yp[3]), "+v"(kvp[0]), "+v"(kvp[1]), "+v"(kvp[2]), "+v"(kvp[3]) :: "memory");
#pragma unroll
        for (int i = 0; i < 4; ++i) {
            const f32x4 y = cacc[i] + yp[i];
            const float k3 = bf_lo(kvp[i].x), k2 = bf_hi(kvp[i].x), k1 = bf_lo(kvp[i].y), k0 = bf_hi(kvp[i].y);
            const unsigned p01 = cvt_pk_bf16(fmaf(k0, dval, y[0]), fmaf(k1, dval, y[1]));
            const unsigned p23 = cvt_pk_bf16(fmaf(k2, dval, y[2]), fmaf(k3, dval, y[3]));
            asm volatile("ds_write_b16 %0, %1 offset:%3\n\tds_write_b16_d16_hi %0, %1 offset:%3+8\n\tds_write_b16 %0, %2 offset:%3+16\n\tds_write_b16_d16_hi %0, %2 offset:%3+24"
                         :: "v"(twa), "v"(p01), "v"(p23), "i"((i >> 1) * CV_CH + (i & 1) * 2112) : "memory");
        }
        WAIT_L(0); BAR();
        STAGEB(2);
        RD64(kvp[0], tra, 0); RD64(kvp[1], tra, 8); RD64(kvp[2], tra, 2 * CV_CH); RD64(kvp[3], tra, 2 * CV_CH + 8);
        asm volatile("s_waitcnt lgkmcnt(0)" : "+v"(kvp[0]), "+v"(kvp[1]), "+v"(kvp[2]), "+v"(kvp[3]) :: "memory");
        __builtin_amdgcn_raw_buffer_store_b128((u32x4){kvp[0].x, kvp[0].y, kvp[1].x, kvp[1].y}, yrs, tid * 16, 0, 16);
        __builtin_amdgcn_raw_buffer_store_b128((u32x4){kvp[2].x, kvp[2].y, kvp[3].x, kvp[3].y}, yrs, 8192 + tid * 16, 0, 16);
#undef conv_issue
#undef conv_wait
#undef conv_mma
    }
#pragma unroll
    for (int n = 0; n < 8; ++n) RDB(0, n);
    wait_vm<8>();

    auto trip = [&](auto tc) __attribute__((always_inline)) {
        constexpr int t = decltype(tc)::value;
        asm volatile("s_waitcnt lgkmcnt(0)" : "+v"(st.B[t & 1][0]), "+v"(st.B[t & 1][1]), "+v"(st.B[t & 1][2]), "+v"(st.B[t & 1][3]), "+v"(st.B[t & 1][4]), "+v"(st.B[t & 1][5]),
                     "+v"(st.B[t & 1][6]), "+v"(st.B[t & 1][7]), "+v"(st.B[t & 1][8]), "+v"(st.B[t & 1][9]), "+v"(st.B[t & 1][10]), "+v"(st.B[t & 1][11]), "+v"(st.B[t & 1][12]),
                     "+v"(st.B[t & 1][13]), "+v"(st.B[t & 1][14]), "+v"(st.B[t & 1][15]), "+v"(st.A[t & 3][0]), "+v"(st.A[t & 3][1]) :: "memory");
        SCHED(); BAR(); SCHED();
        if constexpr (t == CVP) { if (tid == 0) __hip_atomic_store(flags + (FL_C + bid) * 8, epoch, RLX_AGENT); }
        const char* bsrc = cB + (size_t)(t + 4) * kB;
#define MM(n) do { _Pragma("unroll") for (int m = 0; m < 2; ++m) \
            st.acc[m][n] = __builtin_amdgcn_mfma_f32_16x16x32_bf16(CAT8(st.B[t & 1][2 * (n)], st.B[t & 1][2 * (n) + 1]), st.A[t & 3][m], st.acc[m][n], 0, 0, 0); SCHED(); } while (0)
#define RN(n) do { if constexpr (t + 1 < NT2) { RDB(t + 1, n); RDB(t + 1, (n) + 1); } SCHED(); } while (0)
        RN(0); MM(0); RN(2); MM(1); RN(4); MM(2); RN(6); MM(3);
        if constexpr (vm_b(t) != 0) GLDS16(bsrc + voffB[0], RB + rslot(t + 4) + ldsw);
        SCHED(); MM(4);
        if constexpr (vm_b(t) != 0) GLDS16(bsrc + voffB[1], RB + rslot(t + 4) + ldsw + 4096);
        SCHED(); MM(5);
        if constexpr (vm_a(t) != 0) asm volatile("global_load_dwordx4 %0, %1, %2 sc1" : "=v"(st.A[(t + 3) & 3][0]) : "v"(voffA), "s"(cA0 + (size_t)(t + 3) * 2048) : "memory");
        SCHED(); MM(6);
        if constexpr (vm_a(t) != 0) asm volatile("global_load_dwordx4 %0, %1, %2 sc1" : "=v"(st.A[(t + 3) & 3][1]) : "v"(voffA), "s"(cA1 + (size_t)(t + 3) * 2048) : "memory");
        SCHED(); MM(7);
#undef MM
#undef RN
        if constexpr (t + 1 < NT2) wait_vm<vm_wait(t)>();
        SCHED();
    };
#define TRIP4(t) trip(std::integral_constant<int, (t)>{}); trip(std::integral_constant<int, (t) + 1>{}); trip(std::integral_constant<int, (t) + 2>{}); trip(std::integral_constant<int, (t) + 3>{})
    TRIP4(0); TRIP4(4); TRIP4(8); TRIP4(12); TRIP4(16); TRIP4(20); TRIP4(24);
    if (z != kh) {
#pragma unroll
        for (int n = 0; n < 8; ++n) { const f32x4 v = st.acc[0][n]; st.acc[0][n] = st.acc[1][n]; st.acc[1][n] = v; }
    }
#pragma unroll
    for (int n = 0; n < 8; ++n) *(LAS f32x4*)(lds + wid * 8192 + lane * 16 + n * 1024) = st.acc[1][n];
    if (kh) {
        WAIT_L(0); BAR();
        const auto xs = __builtin_amdgcn_make_buffer_rsrc((void*)(ws + WS_X + (size_t)bid * 32768), 0, 32768, 0x00020000);
#pragma unroll
        for (int n = 0; n < 8; ++n)
            __builtin_amdgcn_raw_buffer_store_b128(__builtin_bit_cast(u32x4, st.acc[0][n] + *(const LAS f32x4*)(lds + wm * 8192 + lane * 16 + n * 1024)), xs,
                                                   wm * 8192 + n * 1024 + lane * 16, 0, 16);
        WAIT_V(0);
        if (lane == 0) __hip_atomic_store(flags + (FL_X + bid * 4 + wm) * 8, epoch, RLX_AGENT);
        if (bid == 0 && wm == 0) {
#pragma unroll 1
            for (int i = 0; i < 4; ++i) wait_flags(flags + (FL_C + i * 64 + lane) * 8, true, epoch);
            if (lane == 0) __hip_atomic_store(flags + FL_E * 8, epoch, RLX_AGENT);
        }
    } else {
        const int s = tm * 128 + wm * 32 + z * 16 + fr, dcol = tn * 128 + 4 * fq;
        gu64* fp = flags + ((lane < 32) ? (FL_C + tn * 32 + lane) : (lane == 32) ? (FL_G + (s >> 6) - (fr >> 6)) : (FL_X + (bid ^ 1) * 4 + wm)) * 8;
        u32x2 qv[8];
#pragma unroll
        for (int n = 0; n < 8; ++n) qv[n] = *(const u32x2*)(q + (size_t)s * D_ + dcol + 16 * n);
        const auto xp = __builtin_amdgcn_make_buffer_rsrc((void*)(ws + WS_X + (size_t)(bid ^ 1) * 32768), 0, 32768, 0x00020000);
        const auto y2 = __builtin_amdgcn_make_buffer_rsrc((void*)(ws + WS_Y2), 0, 256 * S_ * 8, 0x00020000);
        WAIT_L(0); BAR();
        wait_flags(fp, lane <= 32, epoch);
        const float g = __uint_as_float(__hip_atomic_load((gu32*)(ws + WS_G) + s, RLX_AGENT));
        u32x2 yv[8];
#pragma unroll
        for (int n = 0; n < 8; ++n) yv[n] = __builtin_amdgcn_raw_buffer_load_b64(y2, (((dcol >> 2) + 4 * n) * S_ + s) * 8, 0, 16);
#pragma unroll
        for (int n = 0; n < 8; ++n) st.acc[0][n] += *(const LAS f32x4*)(lds + (wm + 4) * 8192 + lane * 16 + n * 1024);
        const float omg = rbf(1.0f - g);
        f32x4 ga[8];
#pragma unroll
        for (int n = 0; n < 8; ++n) {
            ga[n] = (f32x4){bf_lo(qv[n].x), bf_hi(qv[n].x), bf_lo(qv[n].y), bf_hi(qv[n].y)} * (f32x4){bf_lo(yv[n].x), bf_hi(yv[n].x), bf_lo(yv[n].y), bf_hi(yv[n].y)} * g
                    + st.acc[0][n] * omg;
        }
        wait_flags(fp, lane > 32, epoch);
        u32x4 pr[8];
#pragma unroll
        for (int n = 0; n < 8; ++n) pr[n] = __builtin_amdgcn_raw_buffer_load_b128(xp, wm * 8192 + n * 1024 + lane * 16, 0, 16);
#pragma unroll
        for (int n = 0; n < 8; ++n) {
            const f32x4 r = __builtin_bit_cast(f32x4, pr[n]) * omg + ga[n];
            u32x2 o;
            o.x = cvt_pk_bf16(r[0], r[1]); o.y = cvt_pk_bf16(r[2], r[3]);
            *(u32x2*)(out + (size_t)s * D_ + dcol + 16 * n) = o;
        }
    }
#undef STAGEB
#undef LOADA
#undef RDB
#undef RD128
#undef RD64
#undef TRIP4
}

extern "C" void kernel_launch(void* const* d_in, const int* in_sizes, int n_in, void* d_out, int out_size, void* d_ws, size_t ws_size,
                              hipStream_t stream) {
    (void)hipFuncSetAttribute((const void*)k_fused, hipFuncAttributeMaxDynamicSharedMemorySize, F_LDS);
    k_fused<<<256, 512, F_LDS, stream>>>((const u16*)d_in[0], (const u16*)d_in[1], (const u16*)d_in[2], (const u16*)d_in[3], (const u16*)d_in[4], (const u16*)d_in[5],
                                         (const u16*)d_in[7], (const u16*)d_in[8], (const u16*)d_in[9], (const u16*)d_in[10], (unsigned char*)d_ws, (u16*)d_out);
}
```
